```python
import torch

def _softmax(x, axis=-1):
    return torch.softmax(x, dim=axis)
def _gelu(x):
    return torch.nn.functional.gelu(x, approximate="tanh")
def _silu(x):
    return torch.nn.functional.silu(x)
def _sigmoid(x):
    return torch.sigmoid(x)
def _layernorm(x, axis=-1, eps=1e-5):
    m = x.mean(dim=axis, keepdim=True)
    v = x.var(dim=axis, keepdim=True, unbiased=False)
    return (x - m) / torch.sqrt(v + eps)
def _scatter_add(tgt, idx, src, axis=0):
    return tgt.clone().index_add_(axis, idx, src)
def _segment_sum(src, seg, n):
    out = torch.zeros((n,) + src.shape[1:], dtype=src.dtype)
    return out.index_add_(0, seg, src)
def _pad(x, pw):
    flat = tuple(v for pair in reversed(pw) for v in pair)
    return torch.nn.functional.pad(x, flat)

kernel_name = 'recurrent_scan__compressed_kv_cla__glu_gated_geglu__decode'

def setup_inputs():

    _w = (lambda a: (lambda t: t.to(torch.bfloat16) if t.is_floating_point() else t)(torch.as_tensor(a)))
    def _A_setup():
        B, H, Sq, Sk, D, M = 4, 32, 1, 4096, 128, 576
        Dc = 4 * D
        return {
            "x": _w((lambda *s: torch.randn(*s, dtype=torch.bfloat16) * 0.05)(B, Sq, M)),
            "c_kv": _w((lambda *s: torch.randn(*s, dtype=torch.bfloat16) * 1.6)(B, Sk, Dc)),
            "w_q": _w((lambda *s: torch.randn(*s, dtype=torch.bfloat16) * 1.6)(M, H, D) * (M ** -0.5)),
            "w_uk": _w((lambda *s: torch.randn(*s, dtype=torch.bfloat16) * 1.6)(Dc, H, D) * (Dc ** -0.5)),
            "w_uv": _w((lambda *s: torch.randn(*s, dtype=torch.bfloat16) * 1.6)(Dc, H, D) * (Dc ** -0.5)),
            "w_o": _w((lambda *s: torch.randn(*s, dtype=torch.bfloat16) * 1.6)(H, D, M) * ((H * D) ** -0.5)),
        }
    _A_in = _A_setup()

    def _B_setup():
        B, S, Dm, Df = 4, 1, 576, 4608
        return {
            "x": _w((lambda *s: torch.randn(*s, dtype=torch.bfloat16) * 0.05)(B, S, Dm)),
            "w_gate": _w((lambda *s: torch.randn(*s, dtype=torch.bfloat16) * 0.2)(Dm, Df) * (Dm ** -0.5)),
            "w_up": _w((lambda *s: torch.randn(*s, dtype=torch.bfloat16) * 0.2)(Dm, Df) * (Dm ** -0.5)),
            "w_down": _w((lambda *s: torch.randn(*s, dtype=torch.bfloat16) * 0.2)(Df, Dm) * (Df ** -0.5)),
        }
    _B_in = _B_setup()

    return {
        "A_x": _A_in["x"],
        "A_c_kv": _A_in["c_kv"],
        "A_w_q": _A_in["w_q"],
        "A_w_uk": _A_in["w_uk"],
        "A_w_uv": _A_in["w_uv"],
        "A_w_o": _A_in["w_o"],
        "B_x": _B_in["x"],
        "B_w_gate": _B_in["w_gate"],
        "B_w_up": _B_in["w_up"],
        "B_w_down": _B_in["w_down"],
    }

def reference(A_x, A_c_kv, A_w_q, A_w_uk, A_w_uv, A_w_o, B_x, B_w_gate, B_w_up, B_w_down):
    def _A_ref(x, c_kv, w_q, w_uk, w_uv, w_o):
        B, Sq, M = x.shape[0], x.shape[1], x.shape[2]
        Sk = c_kv.shape[1]
        H, D = w_q.shape[1], w_q.shape[2]
        q = torch.einsum("bqm,mhd->bhqd", x, w_q)
        k = torch.einsum("bkc,chd->bhkd", c_kv, w_uk)
        v = torch.einsum("bkc,chd->bhkd", c_kv, w_uv)
        scores = torch.einsum("bhqd,bhkd->bhqk", q, k) * (D ** -0.5)
        off = Sk - Sq
        qi = torch.arange(Sq)[:, None]
        kj = torch.arange(Sk)[None, :]
        cmask = kj <= (qi + off)
        scores = torch.where(cmask[None, None], scores, -1e9)
        p = _softmax(scores, axis=-1)
        ctx = torch.einsum("bhqk,bhkd->bhqd", p, v)
        return torch.einsum("bhqd,hdm->bqm", ctx, w_o)

    def _B_ref(x, w_gate, w_up, w_down):
        g = torch.einsum("bsd,df->bsf", x, w_gate)
        u = torch.einsum("bsd,df->bsf", x, w_up)
        h = _gelu(g) * u
        return torch.einsum("bsf,fd->bsd", h, w_down)

    x = A_x
    xn = _layernorm(x, axis=-1)
    s = _A_ref(xn, A_c_kv, A_w_q, A_w_uk, A_w_uv, A_w_o)
    g = _silu(_B_ref(xn, B_w_gate, B_w_up, B_w_down))
    return x + s * g
```

```cpp
#include <hip/hip_runtime.h>
#include <hip/hip_bf16.h>
#include <stdint.h>

typedef unsigned short u16;
__device__ __forceinline__ float bf2f(u16 v) { return __uint_as_float(((uint32_t)v) << 16); }
__device__ __forceinline__ u16 f2bf(float f) {
    uint32_t u = __float_as_uint(f);
    u += 0x7fff + ((u >> 16) & 1);
    return (u16)(u >> 16);
}
constexpr int B = 4, H = 32, SK = 4096, D = 128, M = 576, DC = 512, DF = 4608;

constexpr int NCH = 32;
constexpr size_t OFF_XN = 0;
constexpr size_t OFF_QA = 4096;
constexpr size_t OFF_PO = OFF_QA + 4 * 32 * 512;
constexpr size_t OFF_ML = OFF_PO + (size_t)4 * 32 * NCH * 512;
constexpr size_t OFF_CTX = OFF_ML + 4 * 32 * NCH * 2;
constexpr size_t OFF_H = OFF_CTX + 4 * 4096;

__device__ void layernorm_to_lds(const u16* x, float* xn  , float* red) {
    int w = threadIdx.x >> 6, l = threadIdx.x & 63;
    float v[9]; float s = 0;
    for (int i = 0; i < 9; i++) { v[i] = bf2f(x[w * M + l + 64 * i]); s += v[i]; }
    for (int o = 32; o; o >>= 1) s += __shfl_xor(s, o);
    float m = s / M; float q = 0;
    for (int i = 0; i < 9; i++) { float d = v[i] - m; q += d * d; }
    for (int o = 32; o; o >>= 1) q += __shfl_xor(q, o);
    float r = 1.0f / sqrtf(q / M + 1e-5f);
    for (int i = 0; i < 9; i++) xn[w * M + l + 64 * i] = bf2f(f2bf((v[i] - m) * r));
    __syncthreads();
}

__global__ __launch_bounds__(256) void k_q(const u16* x, const u16* wq, const u16* wuk, float* ws) {
    __shared__ float xn[4 * M]; __shared__ float q[4 * 128]; __shared__ float part[2][4][128];
    layernorm_to_lds(x, xn, nullptr);
    int h = blockIdx.x; int d = threadIdx.x & 127, kh = threadIdx.x >> 7;
    float a[4] = {0, 0, 0, 0};
    for (int m = kh * 288; m < kh * 288 + 288; m++) {
        float wv = bf2f(wq[(size_t)m * 4096 + h * 128 + d]);
        for (int b = 0; b < 4; b++) a[b] += xn[b * M + m] * wv;
    }
    for (int b = 0; b < 4; b++) part[kh][b][d] = a[b];
    __syncthreads();
    for (int i = threadIdx.x; i < 512; i += 256) q[i] = part[0][i >> 7][i & 127] + part[1][i >> 7][i & 127];
    __syncthreads();
    for (int c = threadIdx.x; c < 512; c += 256) {
        float s[4] = {0, 0, 0, 0};
        for (int dd = 0; dd < 128; dd++) {
            float wv = bf2f(wuk[(size_t)c * 4096 + h * 128 + dd]);
            for (int b = 0; b < 4; b++) s[b] += q[b * 128 + dd] * wv;
        }
        for (int b = 0; b < 4; b++) ws[OFF_QA + (b * 32 + h) * 512 + c] = s[b] * 0.08838834764831845f;
    }
}

__global__ __launch_bounds__(256) void k_ffn1(const u16* x, const u16* wg, const u16* wu, float* ws) {
    __shared__ float xn[4 * M];
    layernorm_to_lds(x, xn, nullptr);
    int f = blockIdx.x * 64 + (threadIdx.x & 63); int b = threadIdx.x >> 6;
    float g = 0, u = 0;
    for (int m = 0; m < M; m++) { g += xn[b * M + m] * bf2f(wg[(size_t)m * DF + f]); u += xn[b * M + m] * bf2f(wu[(size_t)m * DF + f]); }
    float t = tanhf(0.7978845608028654f * (g + 0.044715f * g * g * g));
    ws[OFF_H + b * DF + f] = 0.5f * g * (1.f + t) * u;
}

__global__ __launch_bounds__(256) void k_attn(const u16* ckv, float* ws) {
    __shared__ float S[32][128];
    int b = blockIdx.x >> 5, ch = blockIdx.x & 31;
    const u16* kv = ckv + ((size_t)b * SK + ch * 128) * DC;
    int key = threadIdx.x & 127, hh = threadIdx.x >> 7;
    for (int h = hh * 16; h < hh * 16 + 16; h++) {
        const float* qa = ws + OFF_QA + (b * 32 + h) * 512;
        float s = 0;
        for (int c = 0; c < 512; c++) s += qa[c] * bf2f(kv[key * DC + c]);
        S[h][key] = s;
    }
    __syncthreads();
    if (threadIdx.x < 32) {
        int h = threadIdx.x; float m = -1e30f;
        for (int k = 0; k < 128; k++) m = fmaxf(m, S[h][k]);
        float l = 0;
        for (int k = 0; k < 128; k++) { float e = __expf(S[h][k] - m); S[h][k] = e; l += e; }
        ws[OFF_ML + ((b * 32 + h) * NCH + ch) * 2] = m; ws[OFF_ML + ((b * 32 + h) * NCH + ch) * 2 + 1] = l;
    }
    __syncthreads();
    for (int c = threadIdx.x; c < 512; c += 256) {
        float o[32];
        for (int h = 0; h < 32; h++) o[h] = 0;
        for (int k = 0; k < 128; k++) { float v = bf2f(kv[k * DC + c]);
            #pragma unroll
            for (int h = 0; h < 32; h++) o[h] += S[h][k] * v; }
        #pragma unroll
        for (int h = 0; h < 32; h++) ws[OFF_PO + ((size_t)(b * 32 + h) * NCH + ch) * 512 + c] = o[h];
    }
}

__global__ __launch_bounds__(256) void k_comb(const u16* wuv, float* ws) {
    __shared__ float O[512]; __shared__ float part[2][128];
    int b = blockIdx.x >> 5, h = blockIdx.x & 31;
    const float* ml = ws + OFF_ML + (b * 32 + h) * NCH * 2;
    float m = -1e30f; for (int i = 0; i < NCH; i++) m = fmaxf(m, ml[2 * i]);
    float l = 0; for (int i = 0; i < NCH; i++) l += ml[2 * i + 1] * __expf(ml[2 * i] - m);
    for (int c = threadIdx.x; c < 512; c += 256) { float o = 0;
        for (int i = 0; i < NCH; i++) o += ws[OFF_PO + ((size_t)(b * 32 + h) * NCH + i) * 512 + c] * __expf(ml[2 * i] - m);
        O[c] = o / l; }
    __syncthreads();
    int d = threadIdx.x & 127, kh = threadIdx.x >> 7; float a = 0;
    for (int c = kh * 256; c < kh * 256 + 256; c++) a += O[c] * bf2f(wuv[(size_t)c * 4096 + h * 128 + d]);
    part[kh][d] = a; __syncthreads();
    if (threadIdx.x < 128) ws[OFF_CTX + b * 4096 + h * 128 + d] = part[0][d] + part[1][d];
}

__global__ __launch_bounds__(256) void k_out(const u16* x, const u16* wo, const u16* wd, const float* ws, u16* out) {
    __shared__ float ps[16][4][16], pf[16][4][16];
    int col = blockIdx.x * 16 + (threadIdx.x & 15), kg = threadIdx.x >> 4;
    float s[4] = {0,0,0,0}, f[4] = {0,0,0,0};
    for (int k = kg * 256; k < kg * 256 + 256; k++) { float w = bf2f(wo[(size_t)k * M + col]); for (int b = 0; b < 4; b++) s[b] += ws[OFF_CTX + b * 4096 + k] * w; }
    for (int k = kg * 288; k < kg * 288 + 288; k++) { float w = bf2f(wd[(size_t)k * M + col]); for (int b = 0; b < 4; b++) f[b] += ws[OFF_H + b * DF + k] * w; }
    for (int b = 0; b < 4; b++) { ps[kg][b][threadIdx.x & 15] = s[b]; pf[kg][b][threadIdx.x & 15] = f[b]; }
    __syncthreads();
    if (threadIdx.x < 64) { int b = threadIdx.x >> 4, c = threadIdx.x & 15; float ss = 0, ff = 0;
        for (int g = 0; g < 16; g++) { ss += ps[g][b][c]; ff += pf[g][b][c]; }
        float gate = ff / (1.f + __expf(-ff));
        int cc = blockIdx.x * 16 + c;
        out[b * M + cc] = f2bf(bf2f(x[b * M + cc]) + ss * gate); }
}

extern "C" void kernel_launch(void* const* d_in, const int* in_sizes, int n_in,
                              void* d_out, int out_size, void* d_ws, size_t ws_size,
                              hipStream_t stream) {
    const u16* x = (const u16*)d_in[0]; const u16* ckv = (const u16*)d_in[1];
    const u16* wq = (const u16*)d_in[2]; const u16* wuk = (const u16*)d_in[3];
    const u16* wuv = (const u16*)d_in[4]; const u16* wo = (const u16*)d_in[5];
    const u16* wg = (const u16*)d_in[7]; const u16* wu = (const u16*)d_in[8]; const u16* wd = (const u16*)d_in[9];
    float* ws = (float*)d_ws;
    k_q<<<32, 256, 0, stream>>>(x, wq, wuk, ws);
    k_ffn1<<<72, 256, 0, stream>>>(x, wg, wu, ws);
    k_attn<<<128, 256, 0, stream>>>(ckv, ws);
    k_comb<<<128, 256, 0, stream>>>(wuv, ws);
    k_out<<<36, 256, 0, stream>>>(x, wo, wd, ws, (u16*)d_out);
}
```

```cpp
#include <hip/hip_runtime.h>
#include <hip/hip_bf16.h>
#include <stdint.h>

typedef unsigned short u16;
__device__ __forceinline__ float bf2f(u16 v) { return __uint_as_float(((uint32_t)v) << 16); }
__device__ __forceinline__ u16 f2bf(float f) {
    uint32_t u = __float_as_uint(f);
    u += 0x7fff + ((u >> 16) & 1);
    return (u16)(u >> 16);
}
constexpr int B = 4, H = 32, SK = 4096, D = 128, M = 576, DC = 512, DF = 4608;

constexpr int NCH = 32;
constexpr size_t OFF_XN = 0;
constexpr size_t OFF_QA = 4096;
constexpr size_t OFF_PO = OFF_QA + 4 * 32 * 512;
constexpr size_t OFF_ML = OFF_PO + (size_t)4 * 32 * NCH * 512;
constexpr size_t OFF_CTX = OFF_ML + 4 * 32 * NCH * 2;
constexpr size_t OFF_H = OFF_CTX + 4 * 4096;

__device__ void layernorm_to_lds(const u16* x, float* xn  , float* red) {
    int w = threadIdx.x >> 6, l = threadIdx.x & 63;
    float v[9]; float s = 0;
    for (int i = 0; i < 9; i++) { v[i] = bf2f(x[w * M + l + 64 * i]); s += v[i]; }
    for (int o = 32; o; o >>= 1) s += __shfl_xor(s, o);
    float m = s / M; float q = 0;
    for (int i = 0; i < 9; i++) { float d = v[i] - m; q += d * d; }
    for (int o = 32; o; o >>= 1) q += __shfl_xor(q, o);
    float r = 1.0f / sqrtf(q / M + 1e-5f);
    for (int i = 0; i < 9; i++) xn[w * M + l + 64 * i] = bf2f(f2bf((v[i] - m) * r));
    __syncthreads();
}

__global__ __launch_bounds__(256) void k_q(const u16* x, const u16* wq, const u16* wuk, float* ws) {
    __shared__ float xn[4 * M]; __shared__ float q[4 * 128]; __shared__ float part[2][4][128];
    layernorm_to_lds(x, xn, nullptr);
    int h = blockIdx.x; int d = threadIdx.x & 127, kh = threadIdx.x >> 7;
    float a[4] = {0, 0, 0, 0};
    for (int m = kh * 288; m < kh * 288 + 288; m++) {
        float wv = bf2f(wq[(size_t)m * 4096 + h * 128 + d]);
        for (int b = 0; b < 4; b++) a[b] += xn[b * M + m] * wv;
    }
    for (int b = 0; b < 4; b++) part[kh][b][d] = a[b];
    __syncthreads();
    for (int i = threadIdx.x; i < 512; i += 256) q[i] = part[0][i >> 7][i & 127] + part[1][i >> 7][i & 127];
    __syncthreads();
    for (int c = threadIdx.x; c < 512; c += 256) {
        float s[4] = {0, 0, 0, 0};
        for (int dd = 0; dd < 128; dd++) {
            float wv = bf2f(wuk[(size_t)c * 4096 + h * 128 + dd]);
            for (int b = 0; b < 4; b++) s[b] += q[b * 128 + dd] * wv;
        }
        for (int b = 0; b < 4; b++) ws[OFF_QA + (b * 32 + h) * 512 + c] = s[b] * 0.08838834764831845f;
    }
}

__global__ __launch_bounds__(256) void k_ffn1(const u16* x, const u16* wg, const u16* wu, float* ws) {
    __shared__ float xn[4 * M];
    layernorm_to_lds(x, xn, nullptr);
    int f = blockIdx.x * 64 + (threadIdx.x & 63); int b = threadIdx.x >> 6;
    float g = 0, u = 0;
    for (int m = 0; m < M; m++) { g += xn[b * M + m] * bf2f(wg[(size_t)m * DF + f]); u += xn[b * M + m] * bf2f(wu[(size_t)m * DF + f]); }
    float t = tanhf(0.7978845608028654f * (g + 0.044715f * g * g * g));
    ws[OFF_H + b * DF + f] = 0.5f * g * (1.f + t) * u;
}

__global__ __launch_bounds__(256) void k_attn(const u16* ckv, float* ws) {
    __shared__ float S[32][128];
    int b = blockIdx.x >> 5, ch = blockIdx.x & 31;
    const u16* kv = ckv + ((size_t)b * SK + ch * 128) * DC;
    int key = threadIdx.x & 127, hh = threadIdx.x >> 7;
    for (int h = hh * 16; h < hh * 16 + 16; h++) {
        const float* qa = ws + OFF_QA + (b * 32 + h) * 512;
        float s = 0;
        for (int c = 0; c < 512; c++) s += qa[c] * bf2f(kv[key * DC + c]);
        S[h][key] = s;
    }
    __syncthreads();
    if (threadIdx.x < 32) {
        int h = threadIdx.x; float m = -1e30f;
        for (int k = 0; k < 128; k++) m = fmaxf(m, S[h][k]);
        float l = 0;
        for (int k = 0; k < 128; k++) { float e = __expf(S[h][k] - m); S[h][k] = e; l += e; }
        ws[OFF_ML + ((b * 32 + h) * NCH + ch) * 2] = m; ws[OFF_ML + ((b * 32 + h) * NCH + ch) * 2 + 1] = l;
    }
    __syncthreads();
    for (int c = threadIdx.x; c < 512; c += 256) {
        float o[32];
        for (int h = 0; h < 32; h++) o[h] = 0;
        for (int k = 0; k < 128; k++) { float v = bf2f(kv[k * DC + c]);
            #pragma unroll
            for (int h = 0; h < 32; h++) o[h] += S[h][k] * v; }
        #pragma unroll
        for (int h = 0; h < 32; h++) ws[OFF_PO + ((size_t)(b * 32 + h) * NCH + ch) * 512 + c] = o[h];
    }
}

__global__ __launch_bounds__(256) void k_comb(const u16* wuv, float* ws) {
    __shared__ float O[512]; __shared__ float part[2][128];
    int b = blockIdx.x >> 5, h = blockIdx.x & 31;
    const float* ml = ws + OFF_ML + (b * 32 + h) * NCH * 2;
    float m = -1e30f; for (int i = 0; i < NCH; i++) m = fmaxf(m, ml[2 * i]);
    float l = 0; for (int i = 0; i < NCH; i++) l += ml[2 * i + 1] * __expf(ml[2 * i] - m);
    for (int c = threadIdx.x; c < 512; c += 256) { float o = 0;
        for (int i = 0; i < NCH; i++) o += ws[OFF_PO + ((size_t)(b * 32 + h) * NCH + i) * 512 + c] * __expf(ml[2 * i] - m);
        O[c] = o / l; }
    __syncthreads();
    int d = threadIdx.x & 127, kh = threadIdx.x >> 7; float a = 0;
    for (int c = kh * 256; c < kh * 256 + 256; c++) a += O[c] * bf2f(wuv[(size_t)c * 4096 + h * 128 + d]);
    part[kh][d] = a; __syncthreads();
    if (threadIdx.x < 128) ws[OFF_CTX + b * 4096 + h * 128 + d] = part[0][d] + part[1][d];
}

__global__ __launch_bounds__(256) void k_out(const u16* x, const u16* wo, const u16* wd, const float* ws, u16* out) {
    __shared__ float ps[16][4][16], pf[16][4][16];
    int col = blockIdx.x * 16 + (threadIdx.x & 15), kg = threadIdx.x >> 4;
    float s[4] = {0,0,0,0}, f[4] = {0,0,0,0};
    for (int k = kg * 256; k < kg * 256 + 256; k++) { float w = bf2f(wo[(size_t)k * M + col]); for (int b = 0; b < 4; b++) s[b] += ws[OFF_CTX + b * 4096 + k] * w; }
    for (int k = kg * 288; k < kg * 288 + 288; k++) { float w = bf2f(wd[(size_t)k * M + col]); for (int b = 0; b < 4; b++) f[b] += ws[OFF_H + b * DF + k] * w; }
    for (int b = 0; b < 4; b++) { ps[kg][b][threadIdx.x & 15] = s[b]; pf[kg][b][threadIdx.x & 15] = f[b]; }
    __syncthreads();
    if (threadIdx.x < 64) { int b = threadIdx.x >> 4, c = threadIdx.x & 15; float ss = 0, ff = 0;
        for (int g = 0; g < 16; g++) { ss += ps[g][b][c]; ff += pf[g][b][c]; }
        float gate = ff / (1.f + __expf(-ff));
        int cc = blockIdx.x * 16 + c;
        out[b * M + cc] = f2bf(bf2f(x[b * M + cc]) + ss * gate); }
}

#define AMP 200

typedef uint32_t u32x4 __attribute__((ext_vector_type(4)));
__device__ __forceinline__ uint64_t rt() { return __builtin_amdgcn_s_memrealtime(); }
template <int NT_>
__device__ __forceinline__ u32x4 ld(const u32x4* p) { if (NT_) return __builtin_nontemporal_load(p); else return *p; }
template <int NT_, int WIN>
__global__ __launch_bounds__(512) void k_stream(const u32x4* p1, const u32x4* p2, const u32x4* p3, const u32x4* p4, const u32x4* p5,
   const u32x4* p7, const u32x4* p8, const u32x4* p9, uint64_t* tw) {
    uint64_t t0 = rt();
    int g = blockIdx.x, t = threadIdx.x;
    constexpr int N = 27;
    u32x4 v[N];
    const u32x4* a[N];
    int n = 0;
#define ADD(p, share) { _Pragma("unroll") for (int i = 0; i < (share + 511) / 512; i++) { int o = i * 512 + t; if (o >= share) o = t; a[n++] = p + (size_t)g * share + o; } }
    ADD(p2, 1152) ADD(p3, 1024) ADD(p1, 4096) ADD(p4, 1024) ADD(p5, 1152) ADD(p7, 1296) ADD(p8, 1296) ADD(p9, 1296)
    uint32_t acc = 0;
    #pragma unroll
    for (int i = 0; i < N; i++) {
        v[i] = ld<NT_>(a[i]);
        if (i >= WIN) { acc ^= v[i - WIN].x ^ v[i - WIN].y ^ v[i - WIN].z ^ v[i - WIN].w; }
        __builtin_amdgcn_sched_barrier(0);
    }
    #pragma unroll
    for (int i = (N > WIN ? N - WIN : 0); i < N; i++) acc ^= v[i].x ^ v[i].y ^ v[i].z ^ v[i].w;
    if (acc == 0x12345678) tw[4096 + t] = acc;
    __syncthreads();
    uint64_t t1 = rt();
    if (threadIdx.x == 0) { tw[blockIdx.x * 2] = t0; tw[blockIdx.x * 2 + 1] = t1; }
}
__global__ void k_spin(const uint64_t* tw) {
    __shared__ uint64_t mn[256], mx[256];
    mn[threadIdx.x] = tw[threadIdx.x * 2]; mx[threadIdx.x] = tw[threadIdx.x * 2 + 1];
    __syncthreads();
    if (threadIdx.x == 0) {
        uint64_t a = mn[0], b = mx[0];
        for (int i = 1; i < 256; i++) { a = mn[i] < a ? mn[i] : a; b = mx[i] > b ? mx[i] : b; }
        uint64_t dur = (b - a) * AMP;
        if (dur > 1000000ull) dur = 1000000ull;
        uint64_t s = rt();
        while (rt() - s < dur) __builtin_amdgcn_s_sleep(8);
    }
}

extern "C" void kernel_launch(void* const* d_in, const int* in_sizes, int n_in,
                              void* d_out, int out_size, void* d_ws, size_t ws_size,
                              hipStream_t stream) {
    const u16* x = (const u16*)d_in[0]; const u16* ckv = (const u16*)d_in[1];
    const u16* wq = (const u16*)d_in[2]; const u16* wuk = (const u16*)d_in[3];
    const u16* wuv = (const u16*)d_in[4]; const u16* wo = (const u16*)d_in[5];
    const u16* wg = (const u16*)d_in[7]; const u16* wu = (const u16*)d_in[8]; const u16* wd = (const u16*)d_in[9];
    float* ws = (float*)d_ws;
    uint64_t* tw = (uint64_t*)((char*)d_ws + (128u << 20));
    k_stream<0,4><<<256, 512, 0, stream>>>((const u32x4*)d_in[1], (const u32x4*)d_in[2], (const u32x4*)d_in[3], (const u32x4*)d_in[4], (const u32x4*)d_in[5],
      (const u32x4*)d_in[7], (const u32x4*)d_in[8], (const u32x4*)d_in[9], tw);
    k_spin<<<1, 256, 0, stream>>>(tw);
    k_q<<<32, 256, 0, stream>>>(x, wq, wuk, ws);
    k_ffn1<<<72, 256, 0, stream>>>(x, wg, wu, ws);
    k_attn<<<128, 256, 0, stream>>>(ckv, ws);
    k_comb<<<128, 256, 0, stream>>>(wuv, ws);
    k_out<<<36, 256, 0, stream>>>(x, wo, wd, ws, (u16*)d_out);
}
```

```cpp
#include <hip/hip_runtime.h>
#include <stdint.h>

typedef uint32_t u32;
typedef uint16_t u16;
typedef u32 u32x4 __attribute__((ext_vector_type(4)));
typedef u32 u32x2 __attribute__((ext_vector_type(2)));
typedef float f32x4 __attribute__((ext_vector_type(4)));
typedef float f32x2 __attribute__((ext_vector_type(2)));
typedef __bf16 bf16x8 __attribute__((ext_vector_type(8)));
typedef __bf16 bf16x2 __attribute__((ext_vector_type(2)));
typedef short s16x4 __attribute__((ext_vector_type(4)));
#define LAS __attribute__((address_space(3)))
#define DI __device__ __forceinline__

constexpr u32 WS_E  = 0;
constexpr u32 WS_QA = 4096;
constexpr u32 WS_O  = WS_QA + 4 * 32 * 128 * 16;
constexpr u32 WS_ML = WS_O + 4 * 32 * 16 * 32 * 64;
constexpr u32 WS_SP = WS_ML + 4 * 32 * 32 * 16;
constexpr u32 WS_FP = WS_SP + 128 * 2 * 576 * 4;
constexpr u32 WS_ST = WS_FP + 128 * 4 * 576 * 4;
constexpr u32 L_KV = 0;
constexpr u32 L_QA = 131072;
constexpr u32 L_XN = 147456;
constexpr u32 L_Q  = 152064;
constexpr u32 L_P  = 153088;
constexpr u32 L_WM = 157440;
constexpr u32 L_WS = 157952;
constexpr u32 L_END = 158464;
constexpr u32 L_RO = 0;
constexpr u32 L_RP = 1024;
constexpr u32 L_RC = 9216;
constexpr u32 L_RS = 9728;
constexpr u32 L_RE = 14336;
constexpr u32 L_FR = 18432;
constexpr u32 F_GU = 2896;
constexpr u32 L_FPART = 0;
constexpr u32 L_FH = 23552;
constexpr u32 L_F0 = 24576;
constexpr u32 L_F1 = 33792;

constexpr u32 TAGX = 0xA5A5A5A4u;
constexpr u32 POISON = 0xFFFFFFFFu;
constexpr int SPIN_MAX = 1 << 16;

DI float asf(u32 v) { return __builtin_bit_cast(float, v); }
DI u32 asu(float v) { return __builtin_bit_cast(u32, v); }
DI float bflo(u32 v) { return asf(v << 16); }
DI float bfhi(u32 v) { return asf(v & 0xFFFF0000u); }
DI u32 pk(float a, float b) { f32x2 v = {a, b}; return __builtin_bit_cast(u32, __builtin_convertvector(v, bf16x2)); }
DI u32 umin(u32 a, u32 b) { return a < b ? a : b; }
DI u32 san(u32 a) { return umin(a, 0xFFFFFFFEu); }
DI f32x4 mfma(u32x4 a, u32x4 b, f32x4 c) {
    return __builtin_amdgcn_mfma_f32_16x16x32_bf16(__builtin_bit_cast(bf16x8, a), __builtin_bit_cast(bf16x8, b), c, 0, 0, 0);
}
template <int CTRL> DI float dpp(float v) { return asf(__builtin_amdgcn_update_dpp(0, asu(v), CTRL, 0xf, 0xf, true)); }
#define ROR(n) (0x120 + (n))
#define SHL(n) (0x100 + (n))
DI float sw16_add(float a, float b) { auto r = __builtin_amdgcn_permlane16_swap(asu(a), asu(b), false, false); return asf(r[0]) + asf(r[1]); }
DI float sw32_add(float a, float b) { auto r = __builtin_amdgcn_permlane32_swap(asu(a), asu(b), false, false); return asf(r[0]) + asf(r[1]); }
DI float sw16_max(float a) { auto r = __builtin_amdgcn_permlane16_swap(asu(a), asu(a), false, false); return fmaxf(asf(r[0]), asf(r[1])); }
DI float sw32_max(float a) { auto r = __builtin_amdgcn_permlane32_swap(asu(a), asu(a), false, false); return fmaxf(asf(r[0]), asf(r[1])); }
DI float row_sum(float v) { v += dpp<ROR(8)>(v); v += dpp<ROR(4)>(v); v += dpp<ROR(2)>(v); v += dpp<ROR(1)>(v); return v; }
DI float row_max(float v) { v = fmaxf(v, dpp<ROR(8)>(v)); v = fmaxf(v, dpp<ROR(4)>(v)); v = fmaxf(v, dpp<ROR(2)>(v)); v = fmaxf(v, dpp<ROR(1)>(v)); return v; }
DI float wave_sum(float v) { v = row_sum(v); v = sw16_add(v, v); v = sw32_add(v, v); return v; }

template <int OFF = 0> DI void gld(u32x4& v, const void* sb, u32 vo) { asm volatile("global_load_dwordx4 %0, %1, %2 offset:%3" : "=v"(v) : "v"(vo), "s"(sb), "n"(OFF) : "memory"); }
template <int OFF = 0> DI void gldc(u32x4& v, const void* sb, u32 vo) { asm volatile("global_load_dwordx4 %0, %1, %2 offset:%3 sc1" : "=v"(v) : "v"(vo), "s"(sb), "n"(OFF) : "memory"); }
DI void gldc1(u32& v, const void* sb, u32 vo) { asm volatile("global_load_dword %0, %1, %2 sc1" : "=v"(v) : "v"(vo), "s"(sb) : "memory"); }
DI void gld_u16(u32& v, const void* sb, u32 vo) { asm volatile("global_load_ushort %0, %1, %2" : "=v"(v) : "v"(vo), "s"(sb) : "memory"); }
template <int OFF = 0> DI void gstc(const void* sb, u32 vo, u32x4 v) { asm volatile("global_store_dwordx4 %0, %1, %2 offset:%3 sc1\n\ts_nop 1" ::"v"(vo), "v"(v), "s"(sb), "n"(OFF) : "memory"); }
DI void gstc1(const void* sb, u32 vo, u32 v) { asm volatile("global_store_dword %0, %1, %2 sc1\n\ts_nop 1" ::"v"(vo), "v"(v), "s"(sb) : "memory"); }
DI void gst_u16(const void* sb, u32 vo, u32 v) { asm volatile("global_store_short %0, %1, %2\n\ts_nop 1" ::"v"(vo), "v"(v), "s"(sb) : "memory"); }
DI void dma16(const void* sb, u32 vo, u32 ldst) {
    u32 keep;
    asm volatile("s_mov_b32 %0, m0\n\ts_mov_b32 m0, %3\n\ts_nop 0\n\tglobal_load_lds_dwordx4 %1, %2\n\ts_mov_b32 m0, %0" : "=&s"(keep) : "v"(vo), "s"(sb), "s"(ldst) : "memory");
}
#define WAITV(N) asm volatile("s_waitcnt vmcnt(" #N ")" ::: "memory")
#define WAIT1(N, a) asm volatile("s_waitcnt vmcnt(" #N ")" : "+v"(a)::"memory")
#define WAIT2(N, a, b) asm volatile("s_waitcnt vmcnt(" #N ")" : "+v"(a), "+v"(b)::"memory")
#define WAIT3(N, a, b, c) asm volatile("s_waitcnt vmcnt(" #N ")" : "+v"(a), "+v"(b), "+v"(c)::"memory")
#define WAIT8(N, a) asm volatile("s_waitcnt vmcnt(" #N ")" : "+v"(a[0]), "+v"(a[1]), "+v"(a[2]), "+v"(a[3]), "+v"(a[4]), "+v"(a[5]), "+v"(a[6]), "+v"(a[7])::"memory")
#define TIE2(a, b) asm volatile("" : "+v"(a), "+v"(b)::"memory")
#define TIE8(a) asm volatile("" : "+v"(a[0]), "+v"(a[1]), "+v"(a[2]), "+v"(a[3]), "+v"(a[4]), "+v"(a[5]), "+v"(a[6]), "+v"(a[7])::"memory")
#define BAR() asm volatile("s_waitcnt lgkmcnt(0)\n\ts_barrier" ::: "memory")
#define RAWBAR() asm volatile("s_barrier" ::: "memory")

template <int JJ> DI u32x4 tcol(const u32x4* R) {
    constexpr u32 sel = (JJ & 1) ? 0x07060302u : 0x05040100u;
    u32x4 o;
    o[0] = __builtin_amdgcn_perm(R[1][JJ >> 1], R[0][JJ >> 1], sel);
    o[1] = __builtin_amdgcn_perm(R[3][JJ >> 1], R[2][JJ >> 1], sel);
    o[2] = __builtin_amdgcn_perm(R[5][JJ >> 1], R[4][JJ >> 1], sel);
    o[3] = __builtin_amdgcn_perm(R[7][JJ >> 1], R[6][JJ >> 1], sel);
    return o;
}
template <int JJ> DI u32x4 tcol2(u32x4 r0, u32x4 r1) {
    constexpr u32 sel = (JJ & 1) ? 0x07060302u : 0x05040100u;
    u32x4 o = {__builtin_amdgcn_perm(r1[JJ >> 1], r0[JJ >> 1], sel), 0u, 0u, 0u};
    return o;
}
#define SET_MFMA(acc, A, R) do { \
    acc[0] = mfma(A, tcol<0>(R), acc[0]); acc[1] = mfma(A, tcol<1>(R), acc[1]); acc[2] = mfma(A, tcol<2>(R), acc[2]); acc[3] = mfma(A, tcol<3>(R), acc[3]); \
    acc[4] = mfma(A, tcol<4>(R), acc[4]); acc[5] = mfma(A, tcol<5>(R), acc[5]); acc[6] = mfma(A, tcol<6>(R), acc[6]); acc[7] = mfma(A, tcol<7>(R), acc[7]); } while (0)
#define SET_MFMA2(acc, A, r0, r1) do { \
    acc[0] = mfma(A, tcol2<0>(r0, r1), acc[0]); acc[1] = mfma(A, tcol2<1>(r0, r1), acc[1]); acc[2] = mfma(A, tcol2<2>(r0, r1), acc[2]); acc[3] = mfma(A, tcol2<3>(r0, r1), acc[3]); \
    acc[4] = mfma(A, tcol2<4>(r0, r1), acc[4]); acc[5] = mfma(A, tcol2<5>(r0, r1), acc[5]); acc[6] = mfma(A, tcol2<6>(r0, r1), acc[6]); acc[7] = mfma(A, tcol2<7>(r0, r1), acc[7]); } while (0)
#define ZERO8(acc) do { _Pragma("unroll") for (int _q = 0; _q < 8; _q++) acc[_q] = f32x4{0.f, 0.f, 0.f, 0.f}; } while (0)

#define STAMP(k) do { } while (0)

template <class T> DI T lr(LAS char* L, u32 off) { return *(LAS T*)(L + off); }
template <class T> DI void lw(LAS char* L, u32 off, T v) { *(LAS T*)(L + off) = v; }

__global__ __launch_bounds__(512, 2) void mla_ffn_fused(const u16* __restrict__ x, const u16* __restrict__ ckv, const u16* __restrict__ wq,
        const u16* __restrict__ wuk, const u16* __restrict__ wuv, const u16* __restrict__ wo, const u16* __restrict__ wg,
        const u16* __restrict__ wu, const u16* __restrict__ wd, char* __restrict__ ws, u16* __restrict__ out) {
    extern __shared__ __attribute__((aligned(16))) char smem[];
    LAS char* L = (LAS char*)smem;
    const u32 ldsbase = (u32)(uintptr_t)L;
    const int g = blockIdx.x, t = threadIdx.x;
    const int w = __builtin_amdgcn_readfirstlane(t >> 6), l = t & 63, n = l & 15, g4 = l >> 4;
    const int x8 = g & 7, i = g >> 3;
    const int h = 4 * x8 + (i >> 3), j = i & 7;
    const int ab = x8 >> 1, chunk = (x8 & 1) * 16 + (i >> 1), hh = i & 1;
    const bool isR = hh == 0;
    const int rho = i >> 1;
    const int rh = 4 * x8 + (rho >> 2), rbp = (rho >> 1) & 1, rch = rho & 1, rid = 16 * x8 + rho;
    const int f = 16 * x8 + rho, fbase = 9 * (f >> 1) + 5 * (f & 1), ncb = 5 - (f & 1);
    STAMP(0);

    u32 ep; u32x4 x0, x1;
    gldc1(ep, ws, WS_E);
    { const int lx = l < 36 ? l : l - 36; const u32 xo = (w & 3) * 1152 + lx * 16; gld(x0, x, xo); gld<576>(x1, x, xo); }
    RAWBAR();
    u32x4 uk0, uk1, qa0[8], qa1[8], qe0, qe1;
    { const u32 o = (u32)(64 * j + 8 * w + (n & 7)) * 8192 + h * 256 + 64 * (n >> 3) + 16 * g4;
      gld(uk0, wuk, o); gld<128>(uk1, wuk, o); }
    int ph0 = 32 * w + 8 * g4 + 72 * j; if (ph0 >= 576) ph0 -= 576;
    int ph1 = 256 + 32 * w + 8 * g4 + 72 * j; if (ph1 >= 576) ph1 -= 576;
    int phe = 512 + 8 * w + 72 * j; if (phe >= 576) phe -= 576;
    phe += 2 * g4;
    { const u32 o = (u32)ph0 * 8192 + h * 256 + n * 16;
      #pragma unroll
      for (int r = 0; r < 8; r++) gld(qa0[r], (const char*)wq + r * 8192, o); }
    WAIT3(10, ep, x0, x1);
    {
        float v[16];
        #pragma unroll
        for (int k = 0; k < 4; k++) { v[2 * k] = bflo(x0[k]); v[2 * k + 1] = bfhi(x0[k]); v[8 + 2 * k] = bflo(x1[k]); v[9 + 2 * k] = bfhi(x1[k]); }
        float s = 0.f;
        #pragma unroll
        for (int k = 0; k < 16; k++) s += v[k];
        if (l >= 36) s = 0.f;
        const float mean = wave_sum(s) * (1.f / 576.f);
        float q = 0.f;
        #pragma unroll
        for (int k = 0; k < 16; k++) { v[k] -= mean; q += v[k] * v[k]; }
        if (l >= 36) q = 0.f;
        const float rs = 1.f / sqrtf(wave_sum(q) * (1.f / 576.f) + 1e-5f);
        if (w < 4 && l < 36) {
            u32x4 a, b;
            #pragma unroll
            for (int k = 0; k < 4; k++) { a[k] = pk(v[2 * k] * rs, v[2 * k + 1] * rs); b[k] = pk(v[8 + 2 * k] * rs, v[9 + 2 * k] * rs); }
            lw<u32x4>(L, L_XN + w * 1152 + l * 16, a); lw<u32x4>(L, L_XN + w * 1152 + 576 + l * 16, b);
        }
    }
    BAR();
    STAMP(1);
    { const u32 o = (u32)ph1 * 8192 + h * 256 + n * 16;
      #pragma unroll
      for (int r = 0; r < 8; r++) gld(qa1[r], (const char*)wq + r * 8192, o); }
    { const u32 o = (u32)phe * 8192 + h * 256 + n * 16; gld(qe0, wq, o); gld(qe1, (const char*)wq + 8192, o); }
    const u32 uperm = (g4 >> 1) | ((g4 & 1) << 1);
    const u32 oaddr = WS_O + ((((u32)(ab * 32 + 16 * hh + n) * 16 + 2 * w) * 32 + chunk) * 64) + uperm * 16;
    {
        const u32x4 pz = {POISON, POISON, POISON, POISON};
        gstc(ws, oaddr, pz); gstc<2048>(ws, oaddr, pz);
        const u32 mlo = WS_ML + ((u32)(ab * 32 + 16 * hh + n) * 32 + chunk) * 16;
        u32 a3, a4;
        if (isR) { a3 = WS_SP + rid * 4608 + (t < 288 ? t : t - 288) * 16; a4 = mlo; }
        else { a3 = WS_FP + f * 9216 + t * 16; a4 = (t & 64) ? mlo : WS_FP + f * 9216 + (512 + l) * 16; }
        gstc(ws, a3, pz); gstc(ws, a4, pz);
    }
    f32x4 acc[8];
    ZERO8(acc);
    WAIT8(14, qa0); TIE2(uk0, uk1);
    { const u32x4 A = lr<u32x4>(L, L_XN + (n >> 2) * 1152 + ph0 * 2); SET_MFMA(acc, A, qa0); }
    const char* kvb = (const char*)ckv + ((size_t)(ab * 4096 + chunk * 128) + w) * 1024;
    #pragma unroll
    for (int r = 0; r < 8; r++) dma16(kvb + r * 8192, (u32)((l ^ (((r & 3) << 2) | (r >> 2))) * 16), ldsbase + L_KV + (16 * w + r) * 1024);
    WAIT8(12, qa1); TIE2(qe0, qe1);
    #pragma unroll
    for (int r = 8; r < 16; r++) dma16(kvb + r * 8192, (u32)((l ^ (((r & 3) << 2) | (r >> 2))) * 16), ldsbase + L_KV + (16 * w + r) * 1024);
    { const u32x4 A = lr<u32x4>(L, L_XN + (n >> 2) * 1152 + ph1 * 2); SET_MFMA(acc, A, qa1); }
    { const u32x4 A = {lr<u32>(L, L_XN + (n >> 2) * 1152 + phe * 2), 0u, 0u, 0u}; SET_MFMA2(acc, A, qe0, qe1); }
    { const u32 o = L_QA + (w * 4 + g4) * 512 + n * 32;
      lw<f32x4>(L, o, f32x4{acc[0][0], acc[1][0], acc[2][0], acc[3][0]}); lw<f32x4>(L, o + 16, f32x4{acc[4][0], acc[5][0], acc[6][0], acc[7][0]}); }
    WAITV(16);
    BAR();
    STAMP(2);
    { float s = 0.f;
      #pragma unroll
      for (int k = 0; k < 8; k++) s += lr<float>(L, L_QA + k * 2048 + t * 4);
      const float o = dpp<0xB1>(s);
      if (!(t & 1)) lw<u32>(L, L_Q + t * 2, pk(s, o)); }
    BAR();
    const u32 Ta = ep + 1, Tb = Ta ^ TAGX;
    {
        const u32 qo = L_Q + (n & 3) * 256 + 64 * ((n >> 2) & 1) + 16 * g4;
        f32x4 c = {0.f, 0.f, 0.f, 0.f};
        c = mfma(uk0, lr<u32x4>(L, qo), c);
        c = mfma(uk1, lr<u32x4>(L, qo + 128), c);
        const float sc = 0.08838834764831845f * 1.4426950408889634f;
        float r4[4];
        #pragma unroll
        for (int k = 0; k < 4; k++) { auto p = __builtin_amdgcn_permlane32_swap(asu(c[k]), asu(c[k]), false, false); r4[k] = (c[k] + dpp<SHL(4)>(asf(p[1]))) * sc; }
        const u32x4 unit = {pk(r4[0], r4[1]), Ta, pk(r4[2], r4[3]), Tb};
        if (l < 32 && n < 4) gstc(ws, WS_QA + (((u32)(n * 32 + h) * 128) + 16 * j + 2 * w + g4) * 16, unit);
    }
    STAMP(3);
    u32x4 gq[5];
    const u32 gqo = WS_QA + (((u32)(ab * 32 + 16 * hh + (w >> 1)) * 128) + 64 * (w & 1) + l) * 16;
    const u32 tko = (w < 2) ? WS_QA + (((u32)(ab * 32 + 16 * (1 - hh) + (t >> 3)) * 128) + 16 * (t & 7)) * 16 : gqo;
    gldc(gq[0], ws, gqo); gldc(gq[1], ws + 8192, gqo); gldc(gq[2], ws + 16384, gqo); gldc(gq[3], ws + 24576, gqo); gldc(gq[4], ws, tko);
    u32x4 w6[8];
    {
        const int u5 = n >= 10 ? 2 : (n >= 5 ? 1 : 0); int cb = n - 5 * u5; if (cb > ncb - 1) cb = ncb - 1;
        const int s6 = w < 6 ? w : w - 6;
        const char* p6 = isR ? (const char*)wuv : (w < 6 ? (const char*)wg : (const char*)wu);
        const u32 st6 = isR ? 8192u : 9216u;
        const u32 o = isR ? (u32)(256 * rch + 32 * w + 8 * g4) * 8192 + rh * 256 + n * 16 : (u32)(96 * s6 + 32 * u5 + 8 * g4) * 9216 + (fbase + cb) * 16;
        #pragma unroll
        for (int r = 0; r < 8; r++) gld(w6[r], p6 + r * st6, o);
    }
    asm volatile("s_waitcnt vmcnt(8)" : "+v"(gq[0]), "+v"(gq[1]), "+v"(gq[2]), "+v"(gq[3]), "+v"(gq[4])::"memory");
    for (int it = 0; it < SPIN_MAX; it++) {
        bool ok = true;
        #pragma unroll
        for (int k = 0; k < 5; k++) ok = ok && gq[k][1] == Ta && gq[k][3] == Tb;
        if (__builtin_amdgcn_ballot_w64(!ok) == 0) break;
        gldc(gq[0], ws, gqo); gldc(gq[1], ws + 8192, gqo); gldc(gq[2], ws + 16384, gqo); gldc(gq[3], ws + 24576, gqo); gldc(gq[4], ws, tko);
        asm volatile("s_waitcnt vmcnt(0)" : "+v"(gq[0]), "+v"(gq[1]), "+v"(gq[2]), "+v"(gq[3]), "+v"(gq[4])::"memory");
    }
    { const u32 u = 64 * (w & 1) + l;
      #pragma unroll
      for (int k = 0; k < 4; k++) { const u32 hd = 4 * k + (w >> 1); lw<u32x2>(L, L_QA + hd * 1024 + (((u >> 1) ^ hd) << 4) + 8 * (u & 1), u32x2{gq[k][0], gq[k][2]}); } }
    BAR();
    if (g == 0 && t == 0) gstc1(ws, WS_E, Ta);
    STAMP(4);
    const int pi4 = (g4 == 0) ? 2 : (g4 == 3 ? 3 : g4 - 1);
    f32x4 s4 = {0.f, 0.f, 0.f, 0.f};
    {
        const int nq = n >> 2, pin = (nq == 0) ? 2 : (nq == 3 ? 3 : nq - 1);
        const u32 kap = 16 * w + 4 * pin + (n & 3), sg = ((n & 3) << 2) | pin;
        f32x4 s4b = {0.f, 0.f, 0.f, 0.f};
        #pragma unroll
        for (int ks = 0; ks < 16; ks += 2) {
            const u32x4 a0 = lr<u32x4>(L, L_KV + kap * 1024 + (ks >> 2) * 256 + (((4 * (ks & 3) + g4) ^ sg) << 4));
            const u32x4 b0 = lr<u32x4>(L, L_QA + n * 1024 + (ks >> 2) * 256 + (((4 * (ks & 3) + g4) ^ n) << 4));
            s4 = mfma(a0, b0, s4);
            const u32x4 a1 = lr<u32x4>(L, L_KV + kap * 1024 + ((ks + 1) >> 2) * 256 + (((4 * ((ks + 1) & 3) + g4) ^ sg) << 4));
            const u32x4 b1 = lr<u32x4>(L, L_QA + n * 1024 + ((ks + 1) >> 2) * 256 + (((4 * ((ks + 1) & 3) + g4) ^ n) << 4));
            s4b = mfma(a1, b1, s4b);
        }
        s4 += s4b;
    }
    { float m = fmaxf(fmaxf(s4[0], s4[1]), fmaxf(s4[2], s4[3])); m = sw16_max(m); m = sw32_max(m);
      if (g4 == 0) lw<float>(L, L_WM + n * 32 + w * 4, m); }
    BAR();
    float mx;
    { const f32x4 a = lr<f32x4>(L, L_WM + n * 32), b = lr<f32x4>(L, L_WM + n * 32 + 16);
      mx = fmaxf(fmaxf(fmaxf(a[0], a[1]), fmaxf(a[2], a[3])), fmaxf(fmaxf(b[0], b[1]), fmaxf(b[2], b[3])));
      float p[4], s = 0.f;
      #pragma unroll
      for (int k = 0; k < 4; k++) { p[k] = __builtin_amdgcn_exp2f(s4[k] - mx); s += p[k]; }
      lw<u32x2>(L, L_P + n * 272 + (16 * w + 4 * pi4) * 2, u32x2{pk(p[0], p[1]), pk(p[2], p[3])});
      s = sw16_add(s, s); s = sw32_add(s, s);
      if (g4 == 0) lw<float>(L, L_WS + n * 32 + w * 4, s); }
    BAR();
    STAMP(5);
    {
        f32x4 o4[4];
        #pragma unroll
        for (int ct = 0; ct < 4; ct++) o4[ct] = f32x4{0.f, 0.f, 0.f, 0.f};
        const u32 q = n >> 2, p = n & 3;
        const u32 xl = (8 * (w & 1)) ^ (p >> 1) ^ (q << 2) ^ ((2 * g4) & 3);
        const u32 vb = L_KV + (8 * g4 + q) * 1024 + (w >> 1) * 256 + 8 * (p & 1);
        #pragma unroll
        for (int ks = 0; ks < 4; ks++) {
            const u32x4 pb = lr<u32x4>(L, L_P + n * 272 + (32 * ks + 8 * g4) * 2);
            #pragma unroll
            for (int ct = 0; ct < 4; ct++) {
                const u32x2 v0 = __builtin_bit_cast(u32x2, __builtin_amdgcn_ds_read_tr16_b64_v4i16((LAS s16x4*)(L + vb + ks * 32768 + ((xl ^ (2 * ct)) << 4))));
                const u32x2 v1 = __builtin_bit_cast(u32x2, __builtin_amdgcn_ds_read_tr16_b64_v4i16((LAS s16x4*)(L + vb + ks * 32768 + 4096 + ((xl ^ (2 * ct) ^ 1) << 4))));
                o4[ct] = mfma(u32x4{v0[0], v0[1], v1[0], v1[1]}, pb, o4[ct]);
            }
        }
        #pragma unroll
        for (int a = 0; a < 2; a++) {
            const u32 ax = pk(o4[2 * a][0], o4[2 * a][1]), ay = pk(o4[2 * a][2], o4[2 * a][3]);
            const u32 bx = pk(o4[2 * a + 1][0], o4[2 * a + 1][1]), by = pk(o4[2 * a + 1][2], o4[2 * a + 1][3]);
            auto sx = __builtin_amdgcn_permlane16_swap(ax, bx, false, false);
            auto sy = __builtin_amdgcn_permlane16_swap(ay, by, false, false);
            const u32x4 unit = {san(sx[0]), san(sy[0]), san(sx[1]), san(sy[1])};
            if (a == 0) gstc(ws, oaddr, unit); else gstc<2048>(ws, oaddr, unit);
        }
    }
    WAIT8(2, w6);
    if (w == 0) {
        const f32x4 a = lr<f32x4>(L, L_WS + n * 32), b = lr<f32x4>(L, L_WS + n * 32 + 16);
        const float s = ((a[0] + a[1]) + (a[2] + a[3])) + ((b[0] + b[1]) + (b[2] + b[3]));
        const u32x4 unit = {san(asu(mx)), san(asu(s)), 0u, 0u};
        if (l < 16) gstc(ws, WS_ML + ((u32)(ab * 32 + 16 * hh + n) * 32 + chunk) * 16, unit);
    }
    STAMP(6);
    if (isR) {
        u32x4 wo0[8], wo1[8], we0, we1, og[4], ml;
        { const u32 o = (u32)(rh * 128 + 32 * (n >> 3) + 8 * g4) * 1152 + 128 * w + 16 * (n & 7);
          #pragma unroll
          for (int r = 0; r < 8; r++) gld(wo0[r], (const char*)wo + r * 1152, o);
          #pragma unroll
          for (int r = 0; r < 8; r++) gld(wo1[r], (const char*)wo + (64 + r) * 1152, o);
          const u32 oe = (u32)(rh * 128 + 16 * w + 4 * g4 + 2 * (n >> 3)) * 1152 + 1024 + 16 * (n & 7);
          gld(we0, wo, oe); gld<1152>(we1, wo, oe); }
        const u32 ogo = WS_O + (((u32)(2 * rbp * 32 + rh) * 16 + 8 * rch + w) * 32) * 64 + l * 16;
        const u32 mlo = WS_ML + (((u32)((2 * rbp + (l >> 5)) * 32 + rh) * 32) + (l & 31)) * 16;
        gldc(og[0], ws, ogo); gldc<1024>(og[1], ws, ogo); gldc(og[2], ws + 1048576, ogo); gldc<1024>(og[3], ws + 1048576, ogo); gldc(ml, ws, mlo);
        RAWBAR();
        asm volatile("s_waitcnt vmcnt(0)" : "+v"(og[0]), "+v"(og[1]), "+v"(og[2]), "+v"(og[3]), "+v"(ml)::"memory");
        TIE8(wo0); TIE8(wo1); TIE2(we0, we1);
        for (int it = 0; it < SPIN_MAX; it++) {
            bool bad = ml[0] == POISON || ml[1] == POISON;
            #pragma unroll
            for (int k = 0; k < 4; k++) bad = bad || og[k][0] == POISON || og[k][1] == POISON || og[k][2] == POISON || og[k][3] == POISON;
            if (__builtin_amdgcn_ballot_w64(bad) == 0) break;
            gldc(og[0], ws, ogo); gldc<1024>(og[1], ws, ogo); gldc(og[2], ws + 1048576, ogo); gldc<1024>(og[3], ws + 1048576, ogo); gldc(ml, ws, mlo);
            asm volatile("s_waitcnt vmcnt(0)" : "+v"(og[0]), "+v"(og[1]), "+v"(og[2]), "+v"(og[3]), "+v"(ml)::"memory");
        }
        STAMP(7);
        {
            const float m = asf(ml[0]);
            float M = row_max(m); M = sw16_max(M);
            const float al = __builtin_amdgcn_exp2f(m - M);
            float Ls = row_sum(al * asf(ml[1])); Ls = sw16_add(Ls, Ls);
            const float scl = al / Ls;
            #pragma unroll
            for (int bq = 0; bq < 2; bq++) {
                const float s0 = asf(__builtin_amdgcn_ds_bpermute((32 * bq + (l >> 2)) * 4, asu(scl)));
                const float s1 = asf(__builtin_amdgcn_ds_bpermute((32 * bq + 16 + (l >> 2)) * 4, asu(scl)));
                float a[8];
                #pragma unroll
                for (int k = 0; k < 4; k++) { a[2 * k] = s0 * bflo(og[2 * bq][k]) + s1 * bflo(og[2 * bq + 1][k]); a[2 * k + 1] = s0 * bfhi(og[2 * bq][k]) + s1 * bfhi(og[2 * bq + 1][k]); }
                float y[4];
                #pragma unroll
                for (int k = 0; k < 4; k++) y[k] = sw32_add(a[k], a[k + 4]);
                float z0 = sw16_add(y[0], y[2]), z1 = sw16_add(y[1], y[3]);
                z0 += dpp<ROR(8)>(z0); z1 += dpp<ROR(8)>(z1); z0 += dpp<ROR(4)>(z0); z1 += dpp<ROR(4)>(z1);
                if ((l & 12) == 0) lw<u32>(L, L_RO + w * 128 + bq * 64 + (8 * (l & 3) + 4 * (l >> 5) + 2 * ((l >> 4) & 1)) * 2, pk(z0, z1));
            }
        }
        ZERO8(acc);
        { const u32x4 A = lr<u32x4>(L, L_RO + w * 128 + ((n >> 2) & 1) * 64 + 16 * g4); SET_MFMA(acc, A, w6); }
        if (g4 < 2) { const u32 o = L_RP + (w * 2 + g4) * 512 + n * 32;
            lw<f32x4>(L, o, f32x4{acc[0][0], acc[1][0], acc[2][0], acc[3][0]}); lw<f32x4>(L, o + 16, f32x4{acc[4][0], acc[5][0], acc[6][0], acc[7][0]}); }
        BAR();
        { const int tt = t & 255; float s = 0.f;
          #pragma unroll
          for (int k = 0; k < 8; k++) s += lr<float>(L, L_RP + k * 1024 + tt * 4);
          const float o = dpp<0xB1>(s);
          if (t < 256 && !(t & 1)) lw<u32>(L, L_RC + t * 2, pk(s, o)); }
        BAR();
        ZERO8(acc);
        { const u32 co = L_RC + ((n >> 2) & 1) * 256 + (32 * (n & 1) + 8 * g4) * 2;
          const u32x4 A0 = lr<u32x4>(L, co), A1 = lr<u32x4>(L, co + 128);
          SET_MFMA(acc, A0, wo0); SET_MFMA(acc, A1, wo1); }
        {
            f32x4 a, b;
            #pragma unroll
            for (int k = 0; k < 4; k++) { a[k] = acc[k][0] + dpp<ROR(8)>(acc[k][1]); b[k] = acc[4 + k][0] + dpp<ROR(8)>(acc[4 + k][1]); }
            if (n < 8 && g4 < 2) { const u32 o = L_RS + g4 * 2304 + (64 * w + 8 * n) * 4; lw<f32x4>(L, o, a); lw<f32x4>(L, o + 16, b); }
        }
        ZERO8(acc);
        { const u32x4 A = {lr<u32>(L, L_RC + ((n >> 2) & 1) * 256 + (16 * w + 4 * g4 + 2 * (n & 1)) * 2), 0u, 0u, 0u};
          SET_MFMA2(acc, A, we0, we1);
          f32x4 a, b;
          #pragma unroll
          for (int k = 0; k < 4; k++) { a[k] = acc[k][0] + dpp<ROR(8)>(acc[k][1]); b[k] = acc[4 + k][0] + dpp<ROR(8)>(acc[4 + k][1]); }
          if (n < 8 && g4 < 2) { const u32 o = L_RE + (w * 2 + g4) * 256 + n * 32; lw<f32x4>(L, o, a); lw<f32x4>(L, o + 16, b); } }
        BAR();
        if (t < 288) {
            const int bq = t >= 144, c4 = t - 144 * bq;
            f32x4 v;
            if (c4 < 128) v = lr<f32x4>(L, L_RS + bq * 2304 + c4 * 16);
            else { v = f32x4{0.f, 0.f, 0.f, 0.f};
                #pragma unroll
                for (int k = 0; k < 8; k++) v += lr<f32x4>(L, L_RE + (k * 2 + bq) * 256 + (c4 - 128) * 16); }
            gstc(ws, WS_SP + rid * 4608 + t * 16, u32x4{san(asu(v[0])), san(asu(v[1])), san(asu(v[2])), san(asu(v[3]))});
        }
        STAMP(8);
        if (rho < 9) {
            const int fin = 9 * x8 + rho, fb = fin / 18, cg = fin - 18 * fb;
            const int p = t >> 3, c4 = t & 7;
            const u32 so = WS_SP + (u32)(16 * (p >> 3) + 4 * ((p >> 1) & 3) + 2 * (fb >> 1) + (p & 1)) * 4608 + (fb & 1) * 2304 + cg * 128 + c4 * 16;
            const u32 fo = WS_FP + (u32)p * 9216 + fb * 2304 + cg * 128 + c4 * 16;
            u32x4 a0, a1, a2; u32 xv;
            gld_u16(xv, x, (fb * 576 + cg * 32 + (t & 31)) * 2);
            gldc(a0, ws, so); gldc(a1, ws, fo); gldc(a2, ws + 64 * 9216, fo);
            asm volatile("s_waitcnt vmcnt(0)" : "+v"(a0), "+v"(a1), "+v"(a2), "+v"(xv)::"memory");
            for (int it = 0; it < SPIN_MAX; it++) {
                bool bad = false;
                #pragma unroll
                for (int k = 0; k < 4; k++) bad = bad || a0[k] == POISON || a1[k] == POISON || a2[k] == POISON;
                if (__builtin_amdgcn_ballot_w64(bad) == 0) break;
                gldc(a0, ws, so); gldc(a1, ws, fo); gldc(a2, ws + 64 * 9216, fo);
                asm volatile("s_waitcnt vmcnt(0)" : "+v"(a0), "+v"(a1), "+v"(a2)::"memory");
            }
            STAMP(9);
            float sv[4], fv[4];
            #pragma unroll
            for (int k = 0; k < 4; k++) { sv[k] = asf(a0[k]); fv[k] = asf(a1[k]) + asf(a2[k]); }
            float y[4];
            #pragma unroll
            for (int k = 0; k < 4; k++) y[k] = sw32_add(sv[k], fv[k]);
            float z0 = sw16_add(y[0], y[2]), z1 = sw16_add(y[1], y[3]);
            z0 += dpp<ROR(8)>(z0); z1 += dpp<ROR(8)>(z1);
            if ((l & 8) == 0) lw<f32x2>(L, L_FR + (w * 2 + (l >> 5)) * 128 + ((l & 7) * 4 + 2 * ((l >> 4) & 1)) * 4, f32x2{z0, z1});
            BAR();
            if (t < 64) {
                float s = 0.f;
                #pragma unroll
                for (int k = 0; k < 8; k++) s += lr<float>(L, L_FR + k * 256 + t * 4);
                auto r = __builtin_amdgcn_permlane32_swap(asu(s), asu(s), false, false);
                const float ss = asf(r[0]), ff = asf(r[1]);
                const float gate = ff / (1.f + __expf(-ff));
                const u32 res = pk(asf(xv << 16) + ss * gate, 0.f) & 0xFFFFu;
                if (t < 32) gst_u16(out, (fb * 576 + cg * 32 + t) * 2, res);
            }
        }
        STAMP(10);
    } else {
        u32x4 ua[8], db[8];
        const int u5 = n >= 10 ? 2 : (n >= 5 ? 1 : 0); int cb = n - 5 * u5; if (cb > ncb - 1) cb = ncb - 1;
        if (w < 4) { const u32 o = (u32)(96 * (2 + w) + 32 * u5 + 8 * g4) * 9216 + (fbase + cb) * 16;
            #pragma unroll
            for (int r = 0; r < 8; r++) gld(ua[r], (const char*)wu + r * 9216, o); }
        BAR();
        if (w != 2 && w != 3) {
            const int rg4 = ncb == 5 ? 4 : 3;
            u32 o;
            if (w >= 4) o = (u32)(8 * fbase + 8 * g4) * 1152 + (16 * (w - 4) + n) * 16;
            else if (w == 0) o = (u32)(8 * fbase + 8 * (n < 8 ? g4 : rg4)) * 1152 + (64 + (n & 7)) * 16;
            else o = (u32)(8 * fbase + 8 * rg4) * 1152 + (16 * g4 + n) * 16;
            #pragma unroll
            for (int r = 0; r < 8; r++) gld(db[r], (const char*)wd + r * 1152, o);
        }
        const int ua5 = (n >> 2) < 2 ? (n >> 2) : 2;
        const bool fvalid = n < 15 && g4 == u5;
        {
            const int s6 = w < 6 ? w : w - 6;
            ZERO8(acc);
            const u32x4 A = lr<u32x4>(L, L_XN + (n & 3) * 1152 + (96 * s6 + 32 * ua5 + 8 * g4) * 2);
            SET_MFMA(acc, A, w6);
            if (fvalid) { const u32 o = L_FPART + ((w < 6 ? 0 : F_GU) + (3 * s6 + u5) * 160 + 8 * (n - 5 * u5)) * 4;
                #pragma unroll
                for (int b = 0; b < 4; b++) { lw<f32x4>(L, o + b * 160, f32x4{acc[0][b], acc[1][b], acc[2][b], acc[3][b]}); lw<f32x4>(L, o + b * 160 + 16, f32x4{acc[4][b], acc[5][b], acc[6][b], acc[7][b]}); } }
        }
        if (w < 4) {
            if (w < 2) { WAITV(8); } else { WAITV(0); }
            TIE8(ua);
            ZERO8(acc);
            const u32x4 A = lr<u32x4>(L, L_XN + (n & 3) * 1152 + (96 * (2 + w) + 32 * ua5 + 8 * g4) * 2);
            SET_MFMA(acc, A, ua);
            if (fvalid) { const u32 o = L_FPART + (F_GU + (3 * (2 + w) + u5) * 160 + 8 * (n - 5 * u5)) * 4;
                #pragma unroll
                for (int b = 0; b < 4; b++) { lw<f32x4>(L, o + b * 160, f32x4{acc[0][b], acc[1][b], acc[2][b], acc[3][b]}); lw<f32x4>(L, o + b * 160 + 16, f32x4{acc[4][b], acc[5][b], acc[6][b], acc[7][b]}); } }
        }
        STAMP(7);
        BAR();
        if (t < 320) {
            const int gu = t & 1, bc = t >> 1;
            float s = 0.f;
            #pragma unroll
            for (int k = 0; k < 18; k++) s += lr<float>(L, L_FPART + (gu * F_GU + k * 160 + bc) * 4);
            const float uu = dpp<0xB1>(s);
            const int b = bc / 40, col = bc - 40 * b;
            const float inner = 0.7978845608028654f * (s + 0.044715f * s * s * s);
            const float e = __expf(2.f * inner);
            const float th = 1.f - 2.f / (e + 1.f);
            float hv = 0.5f * s * (1.f + th) * uu;
            if (col >= 8 * ncb) hv = 0.f;
            if (gu == 0) lw<u16>(L, L_FH + b * 96 + col * 2, (u16)(pk(hv, 0.f) & 0xFFFFu));
        }
        BAR();
        if (w != 2 && w != 3) {
            WAIT8(0, db);
            ZERO8(acc);
            const int b = n >> 2, rg = n & 3;
            const u32x4 hm = lr<u32x4>(L, L_FH + b * 96 + 16 * g4), he = lr<u32x4>(L, L_FH + b * 96 + 64);
            const u32x4 z4 = {0u, 0u, 0u, 0u};
            u32x4 A;
            if (w >= 4) A = hm;
            else if (w == 0) A = rg == 0 ? hm : ((rg == 1 && g4 == 0) ? he : z4);
            else A = (g4 == rg) ? he : z4;
            SET_MFMA(acc, A, db);
            if (w >= 4) { const u32 o = L_F0 + g4 * 2304 + (16 * (w - 4) + n) * 32;
                lw<f32x4>(L, o, f32x4{acc[0][0], acc[1][0], acc[2][0], acc[3][0]}); lw<f32x4>(L, o + 16, f32x4{acc[4][0], acc[5][0], acc[6][0], acc[7][0]}); }
            else if (w == 0) {
                f32x4 a, c;
                #pragma unroll
                for (int k = 0; k < 4; k++) { a[k] = acc[k][0] + dpp<ROR(8)>(acc[k][1]); c[k] = acc[4 + k][0] + dpp<ROR(8)>(acc[4 + k][1]); }
                if (n < 8) { const u32 o = L_F0 + g4 * 2304 + (64 + n) * 32; lw<f32x4>(L, o, a); lw<f32x4>(L, o + 16, c); }
            } else {
                #pragma unroll
                for (int u = 0; u < 4; u++) { const u32 o = L_F1 + g4 * 2048 + (16 * u + n) * 32;
                    lw<f32x4>(L, o, f32x4{acc[0][u], acc[1][u], acc[2][u], acc[3][u]}); lw<f32x4>(L, o + 16, f32x4{acc[4][u], acc[5][u], acc[6][u], acc[7][u]}); }
            }
        }
        BAR();
        {
            const int b = t / 144, c4 = t - 144 * b;
            f32x4 v = lr<f32x4>(L, L_F0 + t * 16);
            if (c4 < 128) v += lr<f32x4>(L, L_F1 + b * 2048 + c4 * 16);
            gstc(ws, WS_FP + f * 9216 + t * 16, u32x4{san(asu(v[0])), san(asu(v[1])), san(asu(v[2])), san(asu(v[3]))});
            if (t < 64) { const int t2 = 512 + t, c5 = t2 - 432;
                f32x4 y = lr<f32x4>(L, L_F0 + t2 * 16);
                if (c5 < 128) y += lr<f32x4>(L, L_F1 + 3 * 2048 + c5 * 16);
                gstc(ws, WS_FP + f * 9216 + t2 * 16, u32x4{san(asu(y[0])), san(asu(y[1])), san(asu(y[2])), san(asu(y[3]))}); }
        }
        STAMP(8);
    }
}

extern "C" void kernel_launch(void* const* d_in, const int* in_sizes, int n_in, void* d_out, int out_size, void* d_ws, size_t ws_size, hipStream_t stream) {
    (void)in_sizes; (void)n_in; (void)out_size; (void)ws_size;
    (void)hipFuncSetAttribute((const void*)mla_ffn_fused, hipFuncAttributeMaxDynamicSharedMemorySize, L_END);
    mla_ffn_fused<<<256, 512, L_END, stream>>>((const u16*)d_in[0], (const u16*)d_in[1], (const u16*)d_in[2], (const u16*)d_in[3], (const u16*)d_in[4],
        (const u16*)d_in[5], (const u16*)d_in[7], (const u16*)d_in[8], (const u16*)d_in[9], (char*)d_ws, (u16*)d_out);
}
```

```cpp
#include <hip/hip_runtime.h>
#include <stdint.h>

typedef uint32_t u32;
typedef uint16_t u16;
typedef u32 u32x4 __attribute__((ext_vector_type(4)));
typedef u32 u32x2 __attribute__((ext_vector_type(2)));
typedef float f32x4 __attribute__((ext_vector_type(4)));
typedef float f32x2 __attribute__((ext_vector_type(2)));
typedef __bf16 bf16x8 __attribute__((ext_vector_type(8)));
typedef __bf16 bf16x2 __attribute__((ext_vector_type(2)));
typedef short s16x4 __attribute__((ext_vector_type(4)));
#define LAS __attribute__((address_space(3)))
#define DI __device__ __forceinline__

constexpr u32 WS_E  = 0;
constexpr u32 WS_QA = 4096;
constexpr u32 WS_O  = WS_QA + 4 * 32 * 128 * 16;
constexpr u32 WS_ML = WS_O + 4 * 32 * 16 * 32 * 64;
constexpr u32 WS_SP = WS_ML + 4 * 32 * 32 * 16;
constexpr u32 WS_FP = WS_SP + 128 * 2 * 576 * 4;
constexpr u32 WS_ST = WS_FP + 128 * 4 * 576 * 4;
constexpr u32 L_KV = 0;
constexpr u32 L_QA = 131072;
constexpr u32 L_XN = 147456;
constexpr u32 L_Q  = 152064;
constexpr u32 L_P  = 153088;
constexpr u32 L_WM = 157440;
constexpr u32 L_WS = 157952;
constexpr u32 L_END = 158464;
constexpr u32 L_RO = 0;
constexpr u32 L_RP = 1024;
constexpr u32 L_RC = 9216;
constexpr u32 L_RS = 9728;
constexpr u32 L_RE = 14336;
constexpr u32 L_FR = 18432;
constexpr u32 F_GU = 2896;
constexpr u32 L_FPART = 0;
constexpr u32 L_FH = 23552;
constexpr u32 L_F0 = 24576;
constexpr u32 L_F1 = 33792;

constexpr u32 TAGX = 0xA5A5A5A4u;
constexpr u32 POISON = 0xFFFFFFFFu;
constexpr int SPIN_MAX = 1 << 16;

DI float asf(u32 v) { return __builtin_bit_cast(float, v); }
DI u32 asu(float v) { return __builtin_bit_cast(u32, v); }
DI float bflo(u32 v) { return asf(v << 16); }
DI float bfhi(u32 v) { return asf(v & 0xFFFF0000u); }
DI u32 pk(float a, float b) { f32x2 v = {a, b}; return __builtin_bit_cast(u32, __builtin_convertvector(v, bf16x2)); }
DI u32 umin(u32 a, u32 b) { return a < b ? a : b; }
DI u32 san(u32 a) { return umin(a, 0xFFFFFFFEu); }
DI f32x4 mfma(u32x4 a, u32x4 b, f32x4 c) {
    return __builtin_amdgcn_mfma_f32_16x16x32_bf16(__builtin_bit_cast(bf16x8, a), __builtin_bit_cast(bf16x8, b), c, 0, 0, 0);
}
template <int CTRL> DI float dpp(float v) { return asf(__builtin_amdgcn_update_dpp(0, asu(v), CTRL, 0xf, 0xf, true)); }
#define ROR(n) (0x120 + (n))
#define SHL(n) (0x100 + (n))
DI float sw16_add(float a, float b) { auto r = __builtin_amdgcn_permlane16_swap(asu(a), asu(b), false, false); return asf(r[0]) + asf(r[1]); }
DI float sw32_add(float a, float b) { auto r = __builtin_amdgcn_permlane32_swap(asu(a), asu(b), false, false); return asf(r[0]) + asf(r[1]); }
DI float sw16_max(float a) { auto r = __builtin_amdgcn_permlane16_swap(asu(a), asu(a), false, false); return fmaxf(asf(r[0]), asf(r[1])); }
DI float sw32_max(float a) { auto r = __builtin_amdgcn_permlane32_swap(asu(a), asu(a), false, false); return fmaxf(asf(r[0]), asf(r[1])); }
DI float row_sum(float v) { v += dpp<ROR(8)>(v); v += dpp<ROR(4)>(v); v += dpp<ROR(2)>(v); v += dpp<ROR(1)>(v); return v; }
DI float row_max(float v) { v = fmaxf(v, dpp<ROR(8)>(v)); v = fmaxf(v, dpp<ROR(4)>(v)); v = fmaxf(v, dpp<ROR(2)>(v)); v = fmaxf(v, dpp<ROR(1)>(v)); return v; }
DI float wave_sum(float v) { v = row_sum(v); v = sw16_add(v, v); v = sw32_add(v, v); return v; }

template <int OFF = 0> DI void gld(u32x4& v, const void* sb, u32 vo) { asm volatile("global_load_dwordx4 %0, %1, %2 offset:%3" : "=v"(v) : "v"(vo), "s"(sb), "n"(OFF) : "memory"); }
template <int OFF = 0> DI void gldc(u32x4& v, const void* sb, u32 vo) { asm volatile("global_load_dwordx4 %0, %1, %2 offset:%3 sc1" : "=v"(v) : "v"(vo), "s"(sb), "n"(OFF) : "memory"); }
DI void gld1(u32& v, const void* sb, u32 vo) { asm volatile("global_load_dword %0, %1, %2" : "=v"(v) : "v"(vo), "s"(sb) : "memory"); }
DI void gld_u16(u32& v, const void* sb, u32 vo) { asm volatile("global_load_ushort %0, %1, %2" : "=v"(v) : "v"(vo), "s"(sb) : "memory"); }
template <int OFF = 0> DI void gstc(const void* sb, u32 vo, u32x4 v) { asm volatile("global_store_dwordx4 %0, %1, %2 offset:%3 sc1\n\ts_nop 1" ::"v"(vo), "v"(v), "s"(sb), "n"(OFF) : "memory"); }
DI void gstc1(const void* sb, u32 vo, u32 v) { asm volatile("global_store_dword %0, %1, %2 sc1\n\ts_nop 1" ::"v"(vo), "v"(v), "s"(sb) : "memory"); }
DI void gst_u16(const void* sb, u32 vo, u32 v) { asm volatile("global_store_short %0, %1, %2\n\ts_nop 1" ::"v"(vo), "v"(v), "s"(sb) : "memory"); }
DI void dma16(const void* sb, u32 vo, u32 ldst) {
    u32 keep;
    asm volatile("s_mov_b32 %0, m0\n\ts_mov_b32 m0, %3\n\ts_nop 0\n\tglobal_load_lds_dwordx4 %1, %2\n\ts_mov_b32 m0, %0" : "=&s"(keep) : "v"(vo), "s"(sb), "s"(ldst) : "memory");
}
#define WAITV(N) asm volatile("s_waitcnt vmcnt(" #N ")" ::: "memory")
#define WAIT1(N, a) asm volatile("s_waitcnt vmcnt(" #N ")" : "+v"(a)::"memory")
#define WAIT2(N, a, b) asm volatile("s_waitcnt vmcnt(" #N ")" : "+v"(a), "+v"(b)::"memory")
#define WAIT3(N, a, b, c) asm volatile("s_waitcnt vmcnt(" #N ")" : "+v"(a), "+v"(b), "+v"(c)::"memory")
#define WAIT8(N, a) asm volatile("s_waitcnt vmcnt(" #N ")" : "+v"(a[0]), "+v"(a[1]), "+v"(a[2]), "+v"(a[3]), "+v"(a[4]), "+v"(a[5]), "+v"(a[6]), "+v"(a[7])::"memory")
#define TIE2(a, b) asm volatile("" : "+v"(a), "+v"(b)::"memory")
#define TIE8(a) asm volatile("" : "+v"(a[0]), "+v"(a[1]), "+v"(a[2]), "+v"(a[3]), "+v"(a[4]), "+v"(a[5]), "+v"(a[6]), "+v"(a[7])::"memory")
#define BAR() asm volatile("s_waitcnt lgkmcnt(0)\n\ts_barrier" ::: "memory")
#define RAWBAR() asm volatile("s_barrier" ::: "memory")

template <int JJ> DI u32x4 tcol(const u32x4* R) {
    constexpr u32 sel = (JJ & 1) ? 0x07060302u : 0x05040100u;
    u32x4 o;
    o[0] = __builtin_amdgcn_perm(R[1][JJ >> 1], R[0][JJ >> 1], sel);
    o[1] = __builtin_amdgcn_perm(R[3][JJ >> 1], R[2][JJ >> 1], sel);
    o[2] = __builtin_amdgcn_perm(R[5][JJ >> 1], R[4][JJ >> 1], sel);
    o[3] = __builtin_amdgcn_perm(R[7][JJ >> 1], R[6][JJ >> 1], sel);
    return o;
}
template <int JJ> DI u32x4 tcol2(u32x4 r0, u32x4 r1) {
    constexpr u32 sel = (JJ & 1) ? 0x07060302u : 0x05040100u;
    u32x4 o = {__builtin_amdgcn_perm(r1[JJ >> 1], r0[JJ >> 1], sel), 0u, 0u, 0u};
    return o;
}
#define SET_MFMA(acc, A, R) do { \
    acc[0] = mfma(A, tcol<0>(R), acc[0]); acc[1] = mfma(A, tcol<1>(R), acc[1]); acc[2] = mfma(A, tcol<2>(R), acc[2]); acc[3] = mfma(A, tcol<3>(R), acc[3]); \
    acc[4] = mfma(A, tcol<4>(R), acc[4]); acc[5] = mfma(A, tcol<5>(R), acc[5]); acc[6] = mfma(A, tcol<6>(R), acc[6]); acc[7] = mfma(A, tcol<7>(R), acc[7]); } while (0)
#define SET_MFMA2(acc, A, r0, r1) do { \
    acc[0] = mfma(A, tcol2<0>(r0, r1), acc[0]); acc[1] = mfma(A, tcol2<1>(r0, r1), acc[1]); acc[2] = mfma(A, tcol2<2>(r0, r1), acc[2]); acc[3] = mfma(A, tcol2<3>(r0, r1), acc[3]); \
    acc[4] = mfma(A, tcol2<4>(r0, r1), acc[4]); acc[5] = mfma(A, tcol2<5>(r0, r1), acc[5]); acc[6] = mfma(A, tcol2<6>(r0, r1), acc[6]); acc[7] = mfma(A, tcol2<7>(r0, r1), acc[7]); } while (0)
#define ZERO8(acc) do { _Pragma("unroll") for (int _q = 0; _q < 8; _q++) acc[_q] = f32x4{0.f, 0.f, 0.f, 0.f}; } while (0)

#define STAMP(k) do { } while (0)

template <class T> DI T lr(LAS char* L, u32 off) { return *(LAS T*)(L + off); }
template <class T> DI void lw(LAS char* L, u32 off, T v) { *(LAS T*)(L + off) = v; }

__global__ __launch_bounds__(512, 2) void mla_ffn_fused(const u16* __restrict__ x, const u16* __restrict__ ckv, const u16* __restrict__ wq,
        const u16* __restrict__ wuk, const u16* __restrict__ wuv, const u16* __restrict__ wo, const u16* __restrict__ wg,
        const u16* __restrict__ wu, const u16* __restrict__ wd, char* __restrict__ ws, u16* __restrict__ out) {
    extern __shared__ __attribute__((aligned(16))) char smem[];
    LAS char* L = (LAS char*)smem;
    const u32 ldsbase = (u32)(uintptr_t)L;
    const int g = blockIdx.x, t = threadIdx.x;
    const int w = __builtin_amdgcn_readfirstlane(t >> 6), l = t & 63, n = l & 15, g4 = l >> 4;
    const int x8 = g & 7, i = g >> 3;
    const int h = 4 * x8 + (i >> 3), j = i & 7;
    const int ab = x8 >> 1, chunk = (x8 & 1) * 16 + (i >> 1), hh = i & 1;
    const bool isR = hh == 0;
    const int rho = i >> 1;
    const int rh = 4 * x8 + (rho >> 2), rbp = (rho >> 1) & 1, rch = rho & 1, rid = 16 * x8 + rho;
    const int f = 16 * x8 + rho, fbase = 9 * (f >> 1) + 5 * (f & 1), ncb = 5 - (f & 1);
    STAMP(0);

    int ph0 = 32 * w + 8 * g4 + 72 * j; if (ph0 >= 576) ph0 -= 576;
    int ph1 = 256 + 32 * w + 8 * g4 + 72 * j; if (ph1 >= 576) ph1 -= 576;
    int phe = 512 + 8 * w + 72 * j; if (phe >= 576) phe -= 576;
    phe += 2 * g4;
    u32 ep, xe; u32x4 xa0, xa1, x0, x1;
    gld1(ep, ws, WS_E);
    { const u32 xb = (n >> 2) * 1152; gld(xa0, x, xb + ph0 * 2); gld(xa1, x, xb + ph1 * 2); gld1(xe, x, xb + phe * 2); }
    { const int lx = l < 36 ? l : l - 36; const u32 xo = (w & 3) * 1152 + lx * 16; gld(x0, x, xo); gld<576>(x1, x, xo); }
    u32x4 uk0, uk1, qa0[8], qa1[8], qe0, qe1;
    { const u32 o = (u32)(64 * j + 8 * w + (n & 7)) * 8192 + h * 256 + 64 * (n >> 3) + 16 * g4;
      gld(uk0, wuk, o); gld<128>(uk1, wuk, o); }
    { const u32 o = (u32)ph0 * 8192 + h * 256 + n * 16;
      #pragma unroll
      for (int r = 0; r < 8; r++) gld(qa0[r], (const char*)wq + r * 8192, o); }
    { const u32 o = (u32)ph1 * 8192 + h * 256 + n * 16;
      #pragma unroll
      for (int r = 0; r < 8; r++) gld(qa1[r], (const char*)wq + r * 8192, o); }
    { const u32 o = (u32)phe * 8192 + h * 256 + n * 16; gld(qe0, wq, o); gld(qe1, (const char*)wq + 8192, o); }
    const u32 uperm = (g4 >> 1) | ((g4 & 1) << 1);
    const u32 oaddr = WS_O + ((((u32)(ab * 32 + 16 * hh + n) * 16 + 2 * w) * 32 + chunk) * 64) + uperm * 16;
    {
        const u32x4 pz = {POISON, POISON, POISON, POISON};
        gstc(ws, oaddr, pz); gstc<2048>(ws, oaddr, pz);
        const u32 mlo = WS_ML + ((u32)(ab * 32 + 16 * hh + n) * 32 + chunk) * 16;
        u32 a3, a4;
        if (isR) { a3 = WS_SP + rid * 4608 + (t < 288 ? t : t - 288) * 16; a4 = mlo; }
        else { a3 = WS_FP + f * 9216 + t * 16; a4 = (t & 64) ? mlo : WS_FP + f * 9216 + (512 + l) * 16; }
        gstc(ws, a3, pz); gstc(ws, a4, pz);
    }
    asm volatile("s_waitcnt vmcnt(24)" : "+v"(ep), "+v"(xe), "+v"(xa0), "+v"(xa1), "+v"(x0), "+v"(x1)::"memory");
    STAMP(11);
    if (w < 4) {
        float v[16];
        #pragma unroll
        for (int k = 0; k < 4; k++) { v[2 * k] = bflo(x0[k]); v[2 * k + 1] = bfhi(x0[k]); v[8 + 2 * k] = bflo(x1[k]); v[9 + 2 * k] = bfhi(x1[k]); }
        float s = 0.f;
        #pragma unroll
        for (int k = 0; k < 16; k++) s += v[k];
        if (l >= 36) s = 0.f;
        const float mean = wave_sum(s) * (1.f / 576.f);
        float q = 0.f;
        #pragma unroll
        for (int k = 0; k < 16; k++) { v[k] -= mean; q += v[k] * v[k]; }
        if (l >= 36) q = 0.f;
        const float rs = 1.f / sqrtf(wave_sum(q) * (1.f / 576.f) + 1e-5f);
        if (l == 0) lw<f32x2>(L, L_WM + w * 8, f32x2{mean, rs});
        if (l < 36) {
            u32x4 a, b;
            #pragma unroll
            for (int k = 0; k < 4; k++) { a[k] = pk(v[2 * k] * rs, v[2 * k + 1] * rs); b[k] = pk(v[8 + 2 * k] * rs, v[9 + 2 * k] * rs); }
            lw<u32x4>(L, L_XN + w * 1152 + l * 16, a); lw<u32x4>(L, L_XN + w * 1152 + 576 + l * 16, b);
        }
    }
    RAWBAR();
    STAMP(1);
    f32x4 acc[8];
    ZERO8(acc);
    const bool onesrow = (n & 3) == 1;
    const u32 ONE2 = 0x3F803F80u;
    WAIT8(14, qa0); TIE2(uk0, uk1);
    STAMP(12);
    { const u32x4 A = {onesrow ? ONE2 : xa0[0], onesrow ? ONE2 : xa0[1], onesrow ? ONE2 : xa0[2], onesrow ? ONE2 : xa0[3]}; SET_MFMA(acc, A, qa0); }
    WAIT8(4, qa1); TIE2(qe0, qe1);
    STAMP(13);
    { const u32x4 A = {onesrow ? ONE2 : xa1[0], onesrow ? ONE2 : xa1[1], onesrow ? ONE2 : xa1[2], onesrow ? ONE2 : xa1[3]}; SET_MFMA(acc, A, qa1); }
    { const u32x4 A = {onesrow ? ONE2 : xe, 0u, 0u, 0u}; SET_MFMA2(acc, A, qe0, qe1); }
    { const u32 o = L_QA + (w * 4 + g4) * 512 + n * 32;
      lw<f32x4>(L, o, f32x4{acc[0][0], acc[1][0], acc[2][0], acc[3][0]}); lw<f32x4>(L, o + 16, f32x4{acc[4][0], acc[5][0], acc[6][0], acc[7][0]});
      if (g4 == 0) { const u32 oc = L_P + w * 512 + n * 32;
        lw<f32x4>(L, oc, f32x4{acc[0][1], acc[1][1], acc[2][1], acc[3][1]}); lw<f32x4>(L, oc + 16, f32x4{acc[4][1], acc[5][1], acc[6][1], acc[7][1]}); } }
    const char* kvb = (const char*)ckv + ((size_t)(ab * 4096 + chunk * 128) + w) * 1024;
    #pragma unroll
    for (int r0 = 0; r0 < 8; r0++) { const int r = (r0 + 8 * hh) & 15;
        dma16(kvb + r * 8192, (u32)((l ^ (((r & 3) << 2) | (r >> 2))) * 16), ldsbase + L_KV + (16 * w + r) * 1024); }
    STAMP(14);
    BAR();
    STAMP(2);
    { float s = 0.f, c = 0.f;
      #pragma unroll
      for (int k = 0; k < 8; k++) { s += lr<float>(L, L_QA + k * 2048 + t * 4); c += lr<float>(L, L_P + k * 512 + (t & 127) * 4); }
      const f32x2 mr = lr<f32x2>(L, L_WM + (t >> 7) * 8);
      s = (s - mr[0] * c) * mr[1];
      const float o = dpp<0xB1>(s);
      if (!(t & 1)) lw<u32>(L, L_Q + t * 2, pk(s, o)); }
    WAITV(8);
    BAR();
    STAMP(15);
    const u32 Ta = ep + 1, Tb = Ta ^ TAGX;
    {
        const u32 qo = L_Q + (n & 3) * 256 + 64 * ((n >> 2) & 1) + 16 * g4;
        f32x4 c = {0.f, 0.f, 0.f, 0.f};
        c = mfma(uk0, lr<u32x4>(L, qo), c);
        c = mfma(uk1, lr<u32x4>(L, qo + 128), c);
        const float sc = 0.08838834764831845f * 1.4426950408889634f;
        float r4[4];
        #pragma unroll
        for (int k = 0; k < 4; k++) { auto p = __builtin_amdgcn_permlane32_swap(asu(c[k]), asu(c[k]), false, false); r4[k] = (c[k] + dpp<SHL(4)>(asf(p[1]))) * sc; }
        const u32x4 unit = {pk(r4[0], r4[1]), Ta, pk(r4[2], r4[3]), Tb};
        if (l < 32 && n < 4) gstc(ws, WS_QA + (((u32)(n * 32 + h) * 128) + 16 * j + 2 * w + g4) * 16, unit);
    }
    STAMP(3);
    #pragma unroll
    for (int r0 = 8; r0 < 16; r0++) { const int r = (r0 + 8 * hh) & 15;
        dma16(kvb + r * 8192, (u32)((l ^ (((r & 3) << 2) | (r >> 2))) * 16), ldsbase + L_KV + (16 * w + r) * 1024); }
    u32x4 gq[5];
    const u32 gqo = WS_QA + (((u32)(ab * 32 + 16 * hh + (w >> 1)) * 128) + 64 * (w & 1) + l) * 16;
    const u32 tko = (w < 2) ? WS_QA + (((u32)(ab * 32 + 16 * (1 - hh) + (t >> 3)) * 128) + 16 * (t & 7)) * 16 : gqo;
    gldc(gq[0], ws, gqo); gldc(gq[1], ws + 8192, gqo); gldc(gq[2], ws + 16384, gqo); gldc(gq[3], ws + 24576, gqo); gldc(gq[4], ws, tko);
    u32x4 w6[8];
    {
        const int u5 = n >= 10 ? 2 : (n >= 5 ? 1 : 0); int cb = n - 5 * u5; if (cb > ncb - 1) cb = ncb - 1;
        const int s6 = w < 6 ? w : w - 6;
        const char* p6 = isR ? (const char*)wuv : (w < 6 ? (const char*)wg : (const char*)wu);
        const u32 st6 = isR ? 8192u : 9216u;
        const u32 o = isR ? (u32)(256 * rch + 32 * w + 8 * g4) * 8192 + rh * 256 + n * 16 : (u32)(96 * s6 + 32 * u5 + 8 * g4) * 9216 + (fbase + cb) * 16;
        #pragma unroll
        for (int r = 0; r < 8; r++) gld(w6[r], p6 + r * st6, o);
    }
    asm volatile("s_waitcnt vmcnt(8)" : "+v"(gq[0]), "+v"(gq[1]), "+v"(gq[2]), "+v"(gq[3]), "+v"(gq[4])::"memory");
    STAMP(16);
    for (int it = 0; it < SPIN_MAX; it++) {
        bool ok = true;
        #pragma unroll
        for (int k = 0; k < 5; k++) ok = ok && gq[k][1] == Ta && gq[k][3] == Tb;
        if (__builtin_amdgcn_ballot_w64(!ok) == 0) break;
        gldc(gq[0], ws, gqo); gldc(gq[1], ws + 8192, gqo); gldc(gq[2], ws + 16384, gqo); gldc(gq[3], ws + 24576, gqo); gldc(gq[4], ws, tko);
        asm volatile("s_waitcnt vmcnt(0)" : "+v"(gq[0]), "+v"(gq[1]), "+v"(gq[2]), "+v"(gq[3]), "+v"(gq[4])::"memory");
    }
    { const u32 u = 64 * (w & 1) + l;
      #pragma unroll
      for (int k = 0; k < 4; k++) { const u32 hd = 4 * k + (w >> 1); lw<u32x2>(L, L_QA + hd * 1024 + (((u >> 1) ^ hd) << 4) + 8 * (u & 1), u32x2{gq[k][0], gq[k][2]}); } }
    BAR();
    if (g == 0 && t == 0) gstc1(ws, WS_E, Ta);
    STAMP(4);
    const int pi4 = (g4 == 0) ? 2 : (g4 == 3 ? 3 : g4 - 1);
    f32x4 s4 = {0.f, 0.f, 0.f, 0.f};
    {
        const int nq = n >> 2, pin = (nq == 0) ? 2 : (nq == 3 ? 3 : nq - 1);
        const u32 kap = 16 * w + 4 * pin + (n & 3), sg = ((n & 3) << 2) | pin;
        f32x4 s4b = {0.f, 0.f, 0.f, 0.f};
        #pragma unroll
        for (int ks = 0; ks < 16; ks += 2) {
            const u32x4 a0 = lr<u32x4>(L, L_KV + kap * 1024 + (ks >> 2) * 256 + (((4 * (ks & 3) + g4) ^ sg) << 4));
            const u32x4 b0 = lr<u32x4>(L, L_QA + n * 1024 + (ks >> 2) * 256 + (((4 * (ks & 3) + g4) ^ n) << 4));
            s4 = mfma(a0, b0, s4);
            const u32x4 a1 = lr<u32x4>(L, L_KV + kap * 1024 + ((ks + 1) >> 2) * 256 + (((4 * ((ks + 1) & 3) + g4) ^ sg) << 4));
            const u32x4 b1 = lr<u32x4>(L, L_QA + n * 1024 + ((ks + 1) >> 2) * 256 + (((4 * ((ks + 1) & 3) + g4) ^ n) << 4));
            s4b = mfma(a1, b1, s4b);
        }
        s4 += s4b;
    }
    { float m = fmaxf(fmaxf(s4[0], s4[1]), fmaxf(s4[2], s4[3])); m = sw16_max(m); m = sw32_max(m);
      if (g4 == 0) lw<float>(L, L_WM + n * 32 + w * 4, m); }
    BAR();
    float mx;
    { const f32x4 a = lr<f32x4>(L, L_WM + n * 32), b = lr<f32x4>(L, L_WM + n * 32 + 16);
      mx = fmaxf(fmaxf(fmaxf(a[0], a[1]), fmaxf(a[2], a[3])), fmaxf(fmaxf(b[0], b[1]), fmaxf(b[2], b[3])));
      float p[4], s = 0.f;
      #pragma unroll
      for (int k = 0; k < 4; k++) { p[k] = __builtin_amdgcn_exp2f(s4[k] - mx); s += p[k]; }
      lw<u32x2>(L, L_P + n * 272 + (16 * w + 4 * pi4) * 2, u32x2{pk(p[0], p[1]), pk(p[2], p[3])});
      s = sw16_add(s, s); s = sw32_add(s, s);
      if (g4 == 0) lw<float>(L, L_WS + n * 32 + w * 4, s); }
    BAR();
    STAMP(5);
    {
        f32x4 o4[4];
        #pragma unroll
        for (int ct = 0; ct < 4; ct++) o4[ct] = f32x4{0.f, 0.f, 0.f, 0.f};
        const u32 q = n >> 2, p = n & 3;
        const u32 xl = (8 * (w & 1)) ^ (p >> 1) ^ (q << 2) ^ ((2 * g4) & 3);
        const u32 vb = L_KV + (8 * g4 + q) * 1024 + (w >> 1) * 256 + 8 * (p & 1);
        #pragma unroll
        for (int ks = 0; ks < 4; ks++) {
            const u32x4 pb = lr<u32x4>(L, L_P + n * 272 + (32 * ks + 8 * g4) * 2);
            #pragma unroll
            for (int ct = 0; ct < 4; ct++) {
                const u32x2 v0 = __builtin_bit_cast(u32x2, __builtin_amdgcn_ds_read_tr16_b64_v4i16((LAS s16x4*)(L + vb + ks * 32768 + ((xl ^ (2 * ct)) << 4))));
                const u32x2 v1 = __builtin_bit_cast(u32x2, __builtin_amdgcn_ds_read_tr16_b64_v4i16((LAS s16x4*)(L + vb + ks * 32768 + 4096 + ((xl ^ (2 * ct) ^ 1) << 4))));
                o4[ct] = mfma(u32x4{v0[0], v0[1], v1[0], v1[1]}, pb, o4[ct]);
            }
        }
        #pragma unroll
        for (int a = 0; a < 2; a++) {
            const u32 ax = pk(o4[2 * a][0], o4[2 * a][1]), ay = pk(o4[2 * a][2], o4[2 * a][3]);
            const u32 bx = pk(o4[2 * a + 1][0], o4[2 * a + 1][1]), by = pk(o4[2 * a + 1][2], o4[2 * a + 1][3]);
            auto sx = __builtin_amdgcn_permlane16_swap(ax, bx, false, false);
            auto sy = __builtin_amdgcn_permlane16_swap(ay, by, false, false);
            const u32x4 unit = {san(sx[0]), san(sy[0]), san(sx[1]), san(sy[1])};
            if (a == 0) gstc(ws, oaddr, unit); else gstc<2048>(ws, oaddr, unit);
        }
    }
    WAIT8(2, w6);
    if (w == 0) {
        const f32x4 a = lr<f32x4>(L, L_WS + n * 32), b = lr<f32x4>(L, L_WS + n * 32 + 16);
        const float s = ((a[0] + a[1]) + (a[2] + a[3])) + ((b[0] + b[1]) + (b[2] + b[3]));
        const u32x4 unit = {san(asu(mx)), san(asu(s)), 0u, 0u};
        if (l < 16) gstc(ws, WS_ML + ((u32)(ab * 32 + 16 * hh + n) * 32 + chunk) * 16, unit);
    }
    STAMP(6);
    if (isR) {
        u32x4 wo0[8], wo1[8], we0, we1, og[4], ml;
        { const u32 o = (u32)(rh * 128 + 32 * (n >> 3) + 8 * g4) * 1152 + 128 * w + 16 * (n & 7);
          #pragma unroll
          for (int r = 0; r < 8; r++) gld(wo0[r], (const char*)wo + r * 1152, o);
          #pragma unroll
          for (int r = 0; r < 8; r++) gld(wo1[r], (const char*)wo + (64 + r) * 1152, o);
          const u32 oe = (u32)(rh * 128 + 16 * w + 4 * g4 + 2 * (n >> 3)) * 1152 + 1024 + 16 * (n & 7);
          gld(we0, wo, oe); gld<1152>(we1, wo, oe); }
        const u32 ogo = WS_O + (((u32)(2 * rbp * 32 + rh) * 16 + 8 * rch + w) * 32) * 64 + l * 16;
        const u32 mlo = WS_ML + (((u32)((2 * rbp + (l >> 5)) * 32 + rh) * 32) + (l & 31)) * 16;
        gldc(og[0], ws, ogo); gldc<1024>(og[1], ws, ogo); gldc(og[2], ws + 1048576, ogo); gldc<1024>(og[3], ws + 1048576, ogo); gldc(ml, ws, mlo);
        RAWBAR();
        asm volatile("s_waitcnt vmcnt(0)" : "+v"(og[0]), "+v"(og[1]), "+v"(og[2]), "+v"(og[3]), "+v"(ml)::"memory");
        TIE8(wo0); TIE8(wo1); TIE2(we0, we1);
        STAMP(17);
        for (int it = 0; it < SPIN_MAX; it++) {
            bool bad = ml[0] == POISON || ml[1] == POISON;
            #pragma unroll
            for (int k = 0; k < 4; k++) bad = bad || og[k][0] == POISON || og[k][1] == POISON || og[k][2] == POISON || og[k][3] == POISON;
            if (__builtin_amdgcn_ballot_w64(bad) == 0) break;
            gldc(og[0], ws, ogo); gldc<1024>(og[1], ws, ogo); gldc(og[2], ws + 1048576, ogo); gldc<1024>(og[3], ws + 1048576, ogo); gldc(ml, ws, mlo);
            asm volatile("s_waitcnt vmcnt(0)" : "+v"(og[0]), "+v"(og[1]), "+v"(og[2]), "+v"(og[3]), "+v"(ml)::"memory");
        }
        STAMP(7);
        {
            const float m = asf(ml[0]);
            float M = row_max(m); M = sw16_max(M);
            const float al = __builtin_amdgcn_exp2f(m - M);
            float Ls = row_sum(al * asf(ml[1])); Ls = sw16_add(Ls, Ls);
            const float scl = al / Ls;
            #pragma unroll
            for (int bq = 0; bq < 2; bq++) {
                const float s0 = asf(__builtin_amdgcn_ds_bpermute((32 * bq + (l >> 2)) * 4, asu(scl)));
                const float s1 = asf(__builtin_amdgcn_ds_bpermute((32 * bq + 16 + (l >> 2)) * 4, asu(scl)));
                float a[8];
                #pragma unroll
                for (int k = 0; k < 4; k++) { a[2 * k] = s0 * bflo(og[2 * bq][k]) + s1 * bflo(og[2 * bq + 1][k]); a[2 * k + 1] = s0 * bfhi(og[2 * bq][k]) + s1 * bfhi(og[2 * bq + 1][k]); }
                float y[4];
                #pragma unroll
                for (int k = 0; k < 4; k++) y[k] = sw32_add(a[k], a[k + 4]);
                float z0 = sw16_add(y[0], y[2]), z1 = sw16_add(y[1], y[3]);
                z0 += dpp<ROR(8)>(z0); z1 += dpp<ROR(8)>(z1); z0 += dpp<ROR(4)>(z0); z1 += dpp<ROR(4)>(z1);
                if ((l & 12) == 0) lw<u32>(L, L_RO + w * 128 + bq * 64 + (8 * (l & 3) + 4 * (l >> 5) + 2 * ((l >> 4) & 1)) * 2, pk(z0, z1));
            }
        }
        ZERO8(acc);
        { const u32x4 A = lr<u32x4>(L, L_RO + w * 128 + ((n >> 2) & 1) * 64 + 16 * g4); SET_MFMA(acc, A, w6); }
        if (g4 < 2) { const u32 o = L_RP + (w * 2 + g4) * 512 + n * 32;
            lw<f32x4>(L, o, f32x4{acc[0][0], acc[1][0], acc[2][0], acc[3][0]}); lw<f32x4>(L, o + 16, f32x4{acc[4][0], acc[5][0], acc[6][0], acc[7][0]}); }
        BAR();
        { const int tt = t & 255; float s = 0.f;
          #pragma unroll
          for (int k = 0; k < 8; k++) s += lr<float>(L, L_RP + k * 1024 + tt * 4);
          const float o = dpp<0xB1>(s);
          if (t < 256 && !(t & 1)) lw<u32>(L, L_RC + t * 2, pk(s, o)); }
        BAR();
        ZERO8(acc);
        { const u32 co = L_RC + ((n >> 2) & 1) * 256 + (32 * (n & 1) + 8 * g4) * 2;
          const u32x4 A0 = lr<u32x4>(L, co), A1 = lr<u32x4>(L, co + 128);
          SET_MFMA(acc, A0, wo0); SET_MFMA(acc, A1, wo1); }
        {
            f32x4 a, b;
            #pragma unroll
            for (int k = 0; k < 4; k++) { a[k] = acc[k][0] + dpp<ROR(8)>(acc[k][1]); b[k] = acc[4 + k][0] + dpp<ROR(8)>(acc[4 + k][1]); }
            if (n < 8 && g4 < 2) { const u32 o = L_RS + g4 * 2304 + (64 * w + 8 * n) * 4; lw<f32x4>(L, o, a); lw<f32x4>(L, o + 16, b); }
        }
        ZERO8(acc);
        { const u32x4 A = {lr<u32>(L, L_RC + ((n >> 2) & 1) * 256 + (16 * w + 4 * g4 + 2 * (n & 1)) * 2), 0u, 0u, 0u};
          SET_MFMA2(acc, A, we0, we1);
          f32x4 a, b;
          #pragma unroll
          for (int k = 0; k < 4; k++) { a[k] = acc[k][0] + dpp<ROR(8)>(acc[k][1]); b[k] = acc[4 + k][0] + dpp<ROR(8)>(acc[4 + k][1]); }
          if (n < 8 && g4 < 2) { const u32 o = L_RE + (w * 2 + g4) * 256 + n * 32; lw<f32x4>(L, o, a); lw<f32x4>(L, o + 16, b); } }
        BAR();
        if (t < 288) {
            const int bq = t >= 144, c4 = t - 144 * bq;
            f32x4 v;
            if (c4 < 128) v = lr<f32x4>(L, L_RS + bq * 2304 + c4 * 16);
            else { v = f32x4{0.f, 0.f, 0.f, 0.f};
                #pragma unroll
                for (int k = 0; k < 8; k++) v += lr<f32x4>(L, L_RE + (k * 2 + bq) * 256 + (c4 - 128) * 16); }
            gstc(ws, WS_SP + rid * 4608 + t * 16, u32x4{san(asu(v[0])), san(asu(v[1])), san(asu(v[2])), san(asu(v[3]))});
        }
        STAMP(8);
        if (rho < 9) {
            const int fin = 9 * x8 + rho, fb = fin / 18, cg = fin - 18 * fb;
            const int p = t >> 3, c4 = t & 7;
            const u32 so = WS_SP + (u32)(16 * (p >> 3) + 4 * ((p >> 1) & 3) + 2 * (fb >> 1) + (p & 1)) * 4608 + (fb & 1) * 2304 + cg * 128 + c4 * 16;
            const u32 fo = WS_FP + (u32)p * 9216 + fb * 2304 + cg * 128 + c4 * 16;
            u32x4 a0, a1, a2; u32 xv;
            gld_u16(xv, x, (fb * 576 + cg * 32 + (t & 31)) * 2);
            gldc(a0, ws, so); gldc(a1, ws, fo); gldc(a2, ws + 64 * 9216, fo);
            asm volatile("s_waitcnt vmcnt(0)" : "+v"(a0), "+v"(a1), "+v"(a2), "+v"(xv)::"memory");
            for (int it = 0; it < SPIN_MAX; it++) {
                bool bad = false;
                #pragma unroll
                for (int k = 0; k < 4; k++) bad = bad || a0[k] == POISON || a1[k] == POISON || a2[k] == POISON;
                if (__builtin_amdgcn_ballot_w64(bad) == 0) break;
                gldc(a0, ws, so); gldc(a1, ws, fo); gldc(a2, ws + 64 * 9216, fo);
                asm volatile("s_waitcnt vmcnt(0)" : "+v"(a0), "+v"(a1), "+v"(a2)::"memory");
            }
            STAMP(9);
            float sv[4], fv[4];
            #pragma unroll
            for (int k = 0; k < 4; k++) { sv[k] = asf(a0[k]); fv[k] = asf(a1[k]) + asf(a2[k]); }
            float y[4];
            #pragma unroll
            for (int k = 0; k < 4; k++) y[k] = sw32_add(sv[k], fv[k]);
            float z0 = sw16_add(y[0], y[2]), z1 = sw16_add(y[1], y[3]);
            z0 += dpp<ROR(8)>(z0); z1 += dpp<ROR(8)>(z1);
            if ((l & 8) == 0) lw<f32x2>(L, L_FR + (w * 2 + (l >> 5)) * 128 + ((l & 7) * 4 + 2 * ((l >> 4) & 1)) * 4, f32x2{z0, z1});
            BAR();
            if (t < 64) {
                float s = 0.f;
                #pragma unroll
                for (int k = 0; k < 8; k++) s += lr<float>(L, L_FR + k * 256 + t * 4);
                auto r = __builtin_amdgcn_permlane32_swap(asu(s), asu(s), false, false);
                const float ss = asf(r[0]), ff = asf(r[1]);
                const float gate = ff / (1.f + __expf(-ff));
                const u32 res = pk(asf(xv << 16) + ss * gate, 0.f) & 0xFFFFu;
                if (t < 32) gst_u16(out, (fb * 576 + cg * 32 + t) * 2, res);
            }
        }
        STAMP(10);
    } else {
        u32x4 ua[8], db[8];
        const int u5 = n >= 10 ? 2 : (n >= 5 ? 1 : 0); int cb = n - 5 * u5; if (cb > ncb - 1) cb = ncb - 1;
        if (w < 4) { const u32 o = (u32)(96 * (2 + w) + 32 * u5 + 8 * g4) * 9216 + (fbase + cb) * 16;
            #pragma unroll
            for (int r = 0; r < 8; r++) gld(ua[r], (const char*)wu + r * 9216, o); }
        BAR();
        if (w != 2 && w != 3) {
            const int rg4 = ncb == 5 ? 4 : 3;
            u32 o;
            if (w >= 4) o = (u32)(8 * fbase + 8 * g4) * 1152 + (16 * (w - 4) + n) * 16;
            else if (w == 0) o = (u32)(8 * fbase + 8 * (n < 8 ? g4 : rg4)) * 1152 + (64 + (n & 7)) * 16;
            else o = (u32)(8 * fbase + 8 * rg4) * 1152 + (16 * g4 + n) * 16;
            #pragma unroll
            for (int r = 0; r < 8; r++) gld(db[r], (const char*)wd + r * 1152, o);
        }
        const int ua5 = (n >> 2) < 2 ? (n >> 2) : 2;
        const bool fvalid = n < 15 && g4 == u5;
        {
            const int s6 = w < 6 ? w : w - 6;
            ZERO8(acc);
            const u32x4 A = lr<u32x4>(L, L_XN + (n & 3) * 1152 + (96 * s6 + 32 * ua5 + 8 * g4) * 2);
            SET_MFMA(acc, A, w6);
            if (fvalid) { const u32 o = L_FPART + ((w < 6 ? 0 : F_GU) + (3 * s6 + u5) * 160 + 8 * (n - 5 * u5)) * 4;
                #pragma unroll
                for (int b = 0; b < 4; b++) { lw<f32x4>(L, o + b * 160, f32x4{acc[0][b], acc[1][b], acc[2][b], acc[3][b]}); lw<f32x4>(L, o + b * 160 + 16, f32x4{acc[4][b], acc[5][b], acc[6][b], acc[7][b]}); } }
        }
        if (w < 4) {
            if (w < 2) { WAITV(8); } else { WAITV(0); }
            TIE8(ua);
            ZERO8(acc);
            const u32x4 A = lr<u32x4>(L, L_XN + (n & 3) * 1152 + (96 * (2 + w) + 32 * ua5 + 8 * g4) * 2);
            SET_MFMA(acc, A, ua);
            if (fvalid) { const u32 o = L_FPART + (F_GU + (3 * (2 + w) + u5) * 160 + 8 * (n - 5 * u5)) * 4;
                #pragma unroll
                for (int b = 0; b < 4; b++) { lw<f32x4>(L, o + b * 160, f32x4{acc[0][b], acc[1][b], acc[2][b], acc[3][b]}); lw<f32x4>(L, o + b * 160 + 16, f32x4{acc[4][b], acc[5][b], acc[6][b], acc[7][b]}); } }
        }
        STAMP(7);
        BAR();
        if (t < 320) {
            const int gu = t & 1, bc = t >> 1;
            float s = 0.f;
            #pragma unroll
            for (int k = 0; k < 18; k++) s += lr<float>(L, L_FPART + (gu * F_GU + k * 160 + bc) * 4);
            const float uu = dpp<0xB1>(s);
            const int b = bc / 40, col = bc - 40 * b;
            const float inner = 0.7978845608028654f * (s + 0.044715f * s * s * s);
            const float e = __expf(2.f * inner);
            const float th = 1.f - 2.f / (e + 1.f);
            float hv = 0.5f * s * (1.f + th) * uu;
            if (col >= 8 * ncb) hv = 0.f;
            if (gu == 0) lw<u16>(L, L_FH + b * 96 + col * 2, (u16)(pk(hv, 0.f) & 0xFFFFu));
        }
        BAR();
        if (w != 2 && w != 3) {
            WAIT8(0, db);
            ZERO8(acc);
            const int b = n >> 2, rg = n & 3;
            const u32x4 hm = lr<u32x4>(L, L_FH + b * 96 + 16 * g4), he = lr<u32x4>(L, L_FH + b * 96 + 64);
            const u32x4 z4 = {0u, 0u, 0u, 0u};
            u32x4 A;
            if (w >= 4) A = hm;
            else if (w == 0) A = rg == 0 ? hm : ((rg == 1 && g4 == 0) ? he : z4);
            else A = (g4 == rg) ? he : z4;
            SET_MFMA(acc, A, db);
            if (w >= 4) { const u32 o = L_F0 + g4 * 2304 + (16 * (w - 4) + n) * 32;
                lw<f32x4>(L, o, f32x4{acc[0][0], acc[1][0], acc[2][0], acc[3][0]}); lw<f32x4>(L, o + 16, f32x4{acc[4][0], acc[5][0], acc[6][0], acc[7][0]}); }
            else if (w == 0) {
                f32x4 a, c;
                #pragma unroll
                for (int k = 0; k < 4; k++) { a[k] = acc[k][0] + dpp<ROR(8)>(acc[k][1]); c[k] = acc[4 + k][0] + dpp<ROR(8)>(acc[4 + k][1]); }
                if (n < 8) { const u32 o = L_F0 + g4 * 2304 + (64 + n) * 32; lw<f32x4>(L, o, a); lw<f32x4>(L, o + 16, c); }
            } else {
                #pragma unroll
                for (int u = 0; u < 4; u++) { const u32 o = L_F1 + g4 * 2048 + (16 * u + n) * 32;
                    lw<f32x4>(L, o, f32x4{acc[0][u], acc[1][u], acc[2][u], acc[3][u]}); lw<f32x4>(L, o + 16, f32x4{acc[4][u], acc[5][u], acc[6][u], acc[7][u]}); }
            }
        }
        BAR();
        {
            const int b = t / 144, c4 = t - 144 * b;
            f32x4 v = lr<f32x4>(L, L_F0 + t * 16);
            if (c4 < 128) v += lr<f32x4>(L, L_F1 + b * 2048 + c4 * 16);
            gstc(ws, WS_FP + f * 9216 + t * 16, u32x4{san(asu(v[0])), san(asu(v[1])), san(asu(v[2])), san(asu(v[3]))});
            if (t < 64) { const int t2 = 512 + t, c5 = t2 - 432;
                f32x4 y = lr<f32x4>(L, L_F0 + t2 * 16);
                if (c5 < 128) y += lr<f32x4>(L, L_F1 + 3 * 2048 + c5 * 16);
                gstc(ws, WS_FP + f * 9216 + t2 * 16, u32x4{san(asu(y[0])), san(asu(y[1])), san(asu(y[2])), san(asu(y[3]))}); }
        }
        STAMP(8);
    }
}

extern "C" void kernel_launch(void* const* d_in, const int* in_sizes, int n_in, void* d_out, int out_size, void* d_ws, size_t ws_size, hipStream_t stream) {
    (void)in_sizes; (void)n_in; (void)out_size; (void)ws_size;
    (void)hipFuncSetAttribute((const void*)mla_ffn_fused, hipFuncAttributeMaxDynamicSharedMemorySize, L_END);
    mla_ffn_fused<<<256, 512, L_END, stream>>>((const u16*)d_in[0], (const u16*)d_in[1], (const u16*)d_in[2], (const u16*)d_in[3], (const u16*)d_in[4],
        (const u16*)d_in[5], (const u16*)d_in[7], (const u16*)d_in[8], (const u16*)d_in[9], (char*)d_ws, (u16*)d_out);
}
```

```cpp
#include <hip/hip_runtime.h>
#include <stdint.h>

typedef uint32_t u32;
typedef uint16_t u16;
typedef u32 u32x4 __attribute__((ext_vector_type(4)));
typedef u32 u32x2 __attribute__((ext_vector_type(2)));
typedef float f32x4 __attribute__((ext_vector_type(4)));
typedef float f32x2 __attribute__((ext_vector_type(2)));
typedef __bf16 bf16x8 __attribute__((ext_vector_type(8)));
typedef __bf16 bf16x2 __attribute__((ext_vector_type(2)));
typedef short s16x4 __attribute__((ext_vector_type(4)));
#define LAS __attribute__((address_space(3)))
#define DI __device__ __forceinline__

constexpr u32 WS_E  = 0;
constexpr u32 WS_QA = 4096;
constexpr u32 WS_O  = WS_QA + 4 * 32 * 128 * 16;
constexpr u32 WS_ML = WS_O + 4 * 32 * 8 * 32 * 128;
constexpr u32 WS_SP = WS_ML + 4 * 32 * 32 * 16;
constexpr u32 WS_FP = WS_SP + 128 * 576 * 16;
constexpr u32 WS_ST = WS_FP + 128 * 1152 * 16;
constexpr u32 L_KV = 0;
constexpr u32 L_QA = 131072;
constexpr u32 L_XN = 147456;
constexpr u32 L_Q  = 152064;
constexpr u32 L_P  = 153088;
constexpr u32 L_WM = 157440;
constexpr u32 L_WS = 157952;
constexpr u32 L_END = 158464;
constexpr u32 L_RO = 0;
constexpr u32 L_RP = 1024;
constexpr u32 L_RC = 9216;
constexpr u32 L_RS = 9728;
constexpr u32 L_RE = 14336;
constexpr u32 L_FR = 18432;
constexpr u32 F_GU = 2896;
constexpr u32 L_FPART = 0;
constexpr u32 L_FH = 23552;
constexpr u32 L_F0 = 24576;
constexpr u32 L_F1 = 33792;

#define CPF 22400
#define KVN 8
#define SA 4
#define SB 4
#define SC 12
#define SD 20
constexpr u32 TAGX = 0xA5A5A5A4u;
constexpr int SPIN_MAX = 1 << 16;

DI float asf(u32 v) { return __builtin_bit_cast(float, v); }
DI u32 asu(float v) { return __builtin_bit_cast(u32, v); }
DI float bflo(u32 v) { return asf(v << 16); }
DI float bfhi(u32 v) { return asf(v & 0xFFFF0000u); }
DI u32 pk(float a, float b) { f32x2 v = {a, b}; return __builtin_bit_cast(u32, __builtin_convertvector(v, bf16x2)); }
DI f32x4 mfma(u32x4 a, u32x4 b, f32x4 c) {
    return __builtin_amdgcn_mfma_f32_16x16x32_bf16(__builtin_bit_cast(bf16x8, a), __builtin_bit_cast(bf16x8, b), c, 0, 0, 0);
}
template <int CTRL> DI float dpp(float v) { return asf(__builtin_amdgcn_update_dpp(0, asu(v), CTRL, 0xf, 0xf, true)); }
#define ROR(n) (0x120 + (n))
#define SHL(n) (0x100 + (n))
DI float sw16_add(float a, float b) { auto r = __builtin_amdgcn_permlane16_swap(asu(a), asu(b), false, false); return asf(r[0]) + asf(r[1]); }
DI float sw32_add(float a, float b) { auto r = __builtin_amdgcn_permlane32_swap(asu(a), asu(b), false, false); return asf(r[0]) + asf(r[1]); }
DI float sw16_max(float a) { auto r = __builtin_amdgcn_permlane16_swap(asu(a), asu(a), false, false); return fmaxf(asf(r[0]), asf(r[1])); }
DI float sw32_max(float a) { auto r = __builtin_amdgcn_permlane32_swap(asu(a), asu(a), false, false); return fmaxf(asf(r[0]), asf(r[1])); }
DI float row_sum(float v) { v += dpp<ROR(8)>(v); v += dpp<ROR(4)>(v); v += dpp<ROR(2)>(v); v += dpp<ROR(1)>(v); return v; }
DI float row_max(float v) { v = fmaxf(v, dpp<ROR(8)>(v)); v = fmaxf(v, dpp<ROR(4)>(v)); v = fmaxf(v, dpp<ROR(2)>(v)); v = fmaxf(v, dpp<ROR(1)>(v)); return v; }
DI float wave_sum(float v) { v = row_sum(v); v = sw16_add(v, v); v = sw32_add(v, v); return v; }

#define POL ""
#define POLKV ""
template <int OFF = 0> DI void gld(u32x4& v, const void* sb, u32 vo) { asm volatile("global_load_dwordx4 %0, %1, %2 offset:%3" POL : "=v"(v) : "v"(vo), "s"(sb), "n"(OFF) : "memory"); }
template <int OFF = 0> DI void gldc(u32x4& v, const void* sb, u32 vo) { asm volatile("global_load_dwordx4 %0, %1, %2 offset:%3 sc1" : "=v"(v) : "v"(vo), "s"(sb), "n"(OFF) : "memory"); }
template <int OFF = 0> DI void gldcm(u32x4& v, const void* sb, u32 vo, uint64_t m) { uint64_t sv;
    asm volatile("s_mov_b64 %1, exec\n\ts_mov_b64 exec, %4\n\tglobal_load_dwordx4 %0, %2, %3 offset:%5 sc1\n\ts_mov_b64 exec, %1" : "+v"(v), "=&s"(sv) : "v"(vo), "s"(sb), "s"(m), "n"(OFF) : "memory"); }
DI void gldm(u32x4& v, const void* sb, u32 vo, uint64_t m) { uint64_t sv;
    asm volatile("s_mov_b64 %1, exec\n\ts_mov_b64 exec, %4\n\tglobal_load_dwordx4 %0, %2, %3" POL "\n\ts_mov_b64 exec, %1" : "=v"(v), "=&s"(sv) : "v"(vo), "s"(sb), "s"(m) : "memory"); }
DI uint64_t badtag(const u32x4& v, u32 Ta, u32 Tb) { return __builtin_amdgcn_ballot_w64(v[1] != Ta || v[3] != Tb); }
DI uint64_t line_mask(uint64_t m) { uint64_t b = m | (m >> 4); b |= b >> 2; b |= b >> 1; b &= 0x0101010101010101ull; return (b << 8) - b; }
DI void gld1(u32& v, const void* sb, u32 vo) { asm volatile("global_load_dword %0, %1, %2" : "=v"(v) : "v"(vo), "s"(sb) : "memory"); }
DI void gld_u16(u32& v, const void* sb, u32 vo) { asm volatile("global_load_ushort %0, %1, %2" : "=v"(v) : "v"(vo), "s"(sb) : "memory"); }
template <int OFF = 0> DI void gstc(const void* sb, u32 vo, u32x4 v) { asm volatile("global_store_dwordx4 %0, %1, %2 offset:%3 sc1\n\ts_nop 1" ::"v"(vo), "v"(v), "s"(sb), "n"(OFF) : "memory"); }
DI void gstc1(const void* sb, u32 vo, u32 v) { asm volatile("global_store_dword %0, %1, %2 sc1\n\ts_nop 1" ::"v"(vo), "v"(v), "s"(sb) : "memory"); }
DI void gst_u16(const void* sb, u32 vo, u32 v) { asm volatile("global_store_short %0, %1, %2\n\ts_nop 1" ::"v"(vo), "v"(v), "s"(sb) : "memory"); }
DI void dma16(const void* sb, u32 vo, u32 ldst) {
    u32 keep;
    asm volatile("s_mov_b32 %0, m0\n\ts_mov_b32 m0, %3\n\ts_nop 0\n\tglobal_load_lds_dwordx4 %1, %2" POLKV "\n\ts_mov_b32 m0, %0" : "=&s"(keep) : "v"(vo), "s"(sb), "s"(ldst) : "memory");
}
#define WAITV(N) asm volatile("s_waitcnt vmcnt(" #N ")" ::: "memory")
#define WAIT1(N, a) asm volatile("s_waitcnt vmcnt(" #N ")" : "+v"(a)::"memory")
#define WAIT2(N, a, b) asm volatile("s_waitcnt vmcnt(" #N ")" : "+v"(a), "+v"(b)::"memory")
#define WAIT3(N, a, b, c) asm volatile("s_waitcnt vmcnt(" #N ")" : "+v"(a), "+v"(b), "+v"(c)::"memory")
#define WAIT8(N, a) asm volatile("s_waitcnt vmcnt(" #N ")" : "+v"(a[0]), "+v"(a[1]), "+v"(a[2]), "+v"(a[3]), "+v"(a[4]), "+v"(a[5]), "+v"(a[6]), "+v"(a[7])::"memory")
#define TIE2(a, b) asm volatile("" : "+v"(a), "+v"(b)::"memory")
#define TIE8(a) asm volatile("" : "+v"(a[0]), "+v"(a[1]), "+v"(a[2]), "+v"(a[3]), "+v"(a[4]), "+v"(a[5]), "+v"(a[6]), "+v"(a[7])::"memory")
#define BAR() asm volatile("s_waitcnt lgkmcnt(0)\n\ts_barrier" ::: "memory")
#define RAWBAR() asm volatile("s_barrier" ::: "memory")

struct Sel { u32 lo, hi; };
#define WAITSEL(N, S) asm volatile("s_waitcnt vmcnt(" #N ")\n\ts_mov_b32 %0, 0x05040100\n\ts_mov_b32 %1, 0x07060302" : "=s"(S.lo), "=s"(S.hi)::"memory")
template <int JJ> DI u32x4 tcol(const u32x4* R, Sel S) {
    const u32 sel = (JJ & 1) ? S.hi : S.lo;
    u32x4 o;
    o[0] = __builtin_amdgcn_perm(R[1][JJ >> 1], R[0][JJ >> 1], sel);
    o[1] = __builtin_amdgcn_perm(R[3][JJ >> 1], R[2][JJ >> 1], sel);
    o[2] = __builtin_amdgcn_perm(R[5][JJ >> 1], R[4][JJ >> 1], sel);
    o[3] = __builtin_amdgcn_perm(R[7][JJ >> 1], R[6][JJ >> 1], sel);
    return o;
}
template <int JJ> DI u32 tcol2(u32x4 r0, u32x4 r1, Sel S) { return __builtin_amdgcn_perm(r1[JJ >> 1], r0[JJ >> 1], (JJ & 1) ? S.hi : S.lo); }
#define SET_MFMA(acc, A, R, S) do { \
    acc[0] = mfma(A, tcol<0>(R, S), acc[0]); acc[1] = mfma(A, tcol<1>(R, S), acc[1]); acc[2] = mfma(A, tcol<2>(R, S), acc[2]); acc[3] = mfma(A, tcol<3>(R, S), acc[3]); \
    acc[4] = mfma(A, tcol<4>(R, S), acc[4]); acc[5] = mfma(A, tcol<5>(R, S), acc[5]); acc[6] = mfma(A, tcol<6>(R, S), acc[6]); acc[7] = mfma(A, tcol<7>(R, S), acc[7]); } while (0)
#define TRANS8(T, R, S) do { T[0] = tcol<0>(R, S); T[1] = tcol<1>(R, S); T[2] = tcol<2>(R, S); T[3] = tcol<3>(R, S); T[4] = tcol<4>(R, S); T[5] = tcol<5>(R, S); T[6] = tcol<6>(R, S); T[7] = tcol<7>(R, S); } while (0)
#define TRANS2(T, r0, r1, S) do { T[0] = tcol2<0>(r0, r1, S); T[1] = tcol2<1>(r0, r1, S); T[2] = tcol2<2>(r0, r1, S); T[3] = tcol2<3>(r0, r1, S); \
    T[4] = tcol2<4>(r0, r1, S); T[5] = tcol2<5>(r0, r1, S); T[6] = tcol2<6>(r0, r1, S); T[7] = tcol2<7>(r0, r1, S); } while (0)
#define PRE_MFMA(acc, A, T) do { _Pragma("unroll") for (int _q = 0; _q < 8; _q++) acc[_q] = mfma(A, T[_q], acc[_q]); } while (0)
#define PRE_MFMA2(acc, A, T) do { _Pragma("unroll") for (int _q = 0; _q < 8; _q++) acc[_q] = mfma(A, u32x4{T[_q], 0u, 0u, 0u}, acc[_q]); } while (0)
#define ZERO8(acc) do { _Pragma("unroll") for (int _q = 0; _q < 8; _q++) acc[_q] = f32x4{0.f, 0.f, 0.f, 0.f}; } while (0)

constexpr u32 L_TOT = L_END;
#define STAMP(k) do { } while (0)

template <class T> DI T lr(LAS char* L, u32 off) { return *(LAS T*)(L + off); }
template <class T> DI void lw(LAS char* L, u32 off, T v) { *(LAS T*)(L + off) = v; }

__global__ __launch_bounds__(512, 2) void mla_ffn_fused(const u16* __restrict__ x, const u16* __restrict__ ckv, const u16* __restrict__ wq,
        const u16* __restrict__ wuk, const u16* __restrict__ wuv, const u16* __restrict__ wo, const u16* __restrict__ wg,
        const u16* __restrict__ wu, const u16* __restrict__ wd, char* __restrict__ ws, u16* __restrict__ out) {
    extern __shared__ __attribute__((aligned(16))) char smem[];
    LAS char* L = (LAS char*)smem;
    const u32 ldsbase = (u32)(uintptr_t)L;
    const int g = blockIdx.x, t = threadIdx.x;
    const int w = __builtin_amdgcn_readfirstlane(t >> 6), l = t & 63, n = l & 15, g4 = l >> 4;
    const int x8 = g & 7, i = g >> 3;
    const int h = 4 * x8 + (i >> 3), j = i & 7;
    const int ab = x8 >> 1, chunk = (x8 & 1) * 16 + (i >> 1), hh = i & 1;
    const bool isR = hh == 0;
    const int rho = i >> 1;
    const int rh = 4 * x8 + (rho >> 2), rbp = (rho >> 1) & 1, rch = rho & 1, rid = 16 * x8 + rho;
    const int f = 16 * x8 + rho, fbase = 9 * (f >> 1) + 5 * (f & 1), ncb = 5 - (f & 1);
    STAMP(0);

    int ph0 = 32 * w + 8 * g4 + 72 * j; if (ph0 >= 576) ph0 -= 576;
    int ph1 = 256 + 32 * w + 8 * g4 + 72 * j; if (ph1 >= 576) ph1 -= 576;
    int phe = 512 + 8 * w + 72 * j; if (phe >= 576) phe -= 576;
    phe += 2 * g4;
    u32 ep, xe; u32x4 xa0, xa1, x0, x1;
    u32 cpf0;
    { uint64_t pc; asm volatile("s_getpc_b64 %0" : "=s"(pc));
      const u32 o = min((u32)(w * 24 + min(l, 23)) * 128u, (u32)CPF);
      asm volatile("global_load_dword %0, %1, %2" : "=&v"(cpf0) : "v"(o), "s"(pc) : "memory"); }
    gld1(ep, ws, WS_E);
    { const u32 xb = (n >> 2) * 1152; gld(xa0, x, xb + ph0 * 2); gld(xa1, x, xb + ph1 * 2); gld1(xe, x, xb + phe * 2); }
    { const int lx = l < 36 ? l : l - 36; const u32 xo = (w & 3) * 1152 + lx * 16; gld(x0, x, xo); gld<576>(x1, x, xo); }
    u32x4 uk0, uk1, qa0[8], qa1[8], qe0, qe1;
    { const u32 o = (u32)(64 * j + 8 * w + (n & 7)) * 8192 + h * 256 + 64 * (n >> 3) + 16 * g4;
      gld(uk0, wuk, o); gld<128>(uk1, wuk, o); }
    { const u32 o = (u32)ph0 * 8192 + h * 256 + n * 16;
      #pragma unroll
      for (int r = 0; r < 8; r++) gld(qa0[r], (const char*)wq + r * 8192, o); }
    { const u32 o = (u32)ph1 * 8192 + h * 256 + n * 16;
      #pragma unroll
      for (int r = 0; r < 8; r++) gld(qa1[r], (const char*)wq + r * 8192, o); }
    { const u32 o = (u32)phe * 8192 + h * 256 + n * 16; gld(qe0, wq, o); gld(qe1, (const char*)wq + 8192, o); }
    asm volatile("s_waitcnt vmcnt(20)" : "+v"(ep), "+v"(xe), "+v"(xa0), "+v"(xa1), "+v"(x0), "+v"(x1)::"memory");
    asm volatile("" : "+v"(cpf0));
    STAMP(11);
    if (w < 4) {
        float v[16];
        #pragma unroll
        for (int k = 0; k < 4; k++) { v[2 * k] = bflo(x0[k]); v[2 * k + 1] = bfhi(x0[k]); v[8 + 2 * k] = bflo(x1[k]); v[9 + 2 * k] = bfhi(x1[k]); }
        float s = 0.f;
        #pragma unroll
        for (int k = 0; k < 16; k++) s += v[k];
        if (l >= 36) s = 0.f;
        const float mean = wave_sum(s) * (1.f / 576.f);
        float q = 0.f;
        #pragma unroll
        for (int k = 0; k < 16; k++) { v[k] -= mean; q += v[k] * v[k]; }
        if (l >= 36) q = 0.f;
        const float rs = 1.f / sqrtf(wave_sum(q) * (1.f / 576.f) + 1e-5f);
        if (l == 0) lw<f32x2>(L, L_WM + w * 8, f32x2{mean, rs});
        if (l < 36) {
            u32x4 a, b;
            #pragma unroll
            for (int k = 0; k < 4; k++) { a[k] = pk(v[2 * k] * rs, v[2 * k + 1] * rs); b[k] = pk(v[8 + 2 * k] * rs, v[9 + 2 * k] * rs); }
            lw<u32x4>(L, L_XN + w * 1152 + l * 16, a); lw<u32x4>(L, L_XN + w * 1152 + 576 + l * 16, b);
        }
    }
    RAWBAR();
    STAMP(1);
    f32x4 acc[8];
    ZERO8(acc);
    const bool onesrow = (n & 3) == 1;
    const u32 ONE2 = 0x3F803F80u;
    Sel S0; WAITSEL(10, S0);
    TIE2(uk0, uk1);
    STAMP(12);
    { const u32x4 A = {onesrow ? ONE2 : xa0[0], onesrow ? ONE2 : xa0[1], onesrow ? ONE2 : xa0[2], onesrow ? ONE2 : xa0[3]}; SET_MFMA(acc, A, qa0, S0); }
    const char* kvb = (const char*)ckv + ((size_t)(ab * 4096 + chunk * 128) + w) * 1024;
    #pragma unroll
    for (int r0 = 0; r0 < KVN; r0++) { const int r = (r0 + 8 * hh) & 15;
        dma16(kvb + r * 8192, (u32)((l ^ (((r & 3) << 2) | (r >> 2))) * 16), ldsbase + L_KV + (16 * w + r) * 1024); }
    Sel S1; asm volatile("s_waitcnt vmcnt(%2)\n\ts_mov_b32 %0, 0x05040100\n\ts_mov_b32 %1, 0x07060302" : "=s"(S1.lo), "=s"(S1.hi) : "n"(KVN) : "memory");
    STAMP(13);
    { const u32x4 A = {onesrow ? ONE2 : xa1[0], onesrow ? ONE2 : xa1[1], onesrow ? ONE2 : xa1[2], onesrow ? ONE2 : xa1[3]}; SET_MFMA(acc, A, qa1, S1); }
    { const u32x4 A = {onesrow ? ONE2 : xe, 0u, 0u, 0u}; u32 TE[8]; TRANS2(TE, qe0, qe1, S1); PRE_MFMA2(acc, A, TE); }
    { const u32 o = L_QA + (w * 4 + g4) * 512 + n * 32;
      lw<f32x4>(L, o, f32x4{acc[0][0], acc[1][0], acc[2][0], acc[3][0]}); lw<f32x4>(L, o + 16, f32x4{acc[4][0], acc[5][0], acc[6][0], acc[7][0]});
      if (g4 == 0) { const u32 oc = L_P + w * 512 + n * 32;
        lw<f32x4>(L, oc, f32x4{acc[0][1], acc[1][1], acc[2][1], acc[3][1]}); lw<f32x4>(L, oc + 16, f32x4{acc[4][1], acc[5][1], acc[6][1], acc[7][1]}); } }
    STAMP(14);
    BAR();
    STAMP(2);
    { float s = 0.f, c = 0.f;
      #pragma unroll
      for (int k = 0; k < 8; k++) { s += lr<float>(L, L_QA + k * 2048 + t * 4); c += lr<float>(L, L_P + k * 512 + (t & 127) * 4); }
      const f32x2 mr = lr<f32x2>(L, L_WM + (t >> 7) * 8);
      s = (s - mr[0] * c) * mr[1];
      const float o = dpp<0xB1>(s);
      if (!(t & 1)) lw<u32>(L, L_Q + t * 2, pk(s, o)); }
    BAR();
    STAMP(15);
    const u32 Ta = ep + 1, Tb = Ta ^ TAGX;
    {
        const u32 qo = L_Q + (n & 3) * 256 + 64 * ((n >> 2) & 1) + 16 * g4;
        f32x4 c = {0.f, 0.f, 0.f, 0.f};
        c = mfma(uk0, lr<u32x4>(L, qo), c);
        c = mfma(uk1, lr<u32x4>(L, qo + 128), c);
        const float sc = 0.08838834764831845f * 1.4426950408889634f;
        float r4[4];
        #pragma unroll
        for (int k = 0; k < 4; k++) { auto p = __builtin_amdgcn_permlane32_swap(asu(c[k]), asu(c[k]), false, false); r4[k] = (c[k] + dpp<SHL(4)>(asf(p[1]))) * sc; }
        const u32x4 unit = {pk(r4[0], r4[1]), Ta, pk(r4[2], r4[3]), Tb};
        if (l < 32 && n < 4) gstc(ws, WS_QA + (((u32)(n * 32 + h) * 128) + 16 * j + 2 * w + g4) * 16, unit);
    }
    STAMP(3);
    #pragma unroll
    for (int r0 = KVN; r0 < 16; r0++) { const int r = (r0 + 8 * hh) & 15;
        dma16(kvb + r * 8192, (u32)((l ^ (((r & 3) << 2) | (r >> 2))) * 16), ldsbase + L_KV + (16 * w + r) * 1024); }
    u32x4 gq[4];
    const u32 gqo = WS_QA + (((u32)(ab * 32 + 16 * hh + (w >> 1)) * 128) + 64 * (w & 1) + l) * 16;
    gldc(gq[0], ws, gqo); gldc(gq[1], ws + 8192, gqo); gldc(gq[2], ws + 16384, gqo); gldc(gq[3], ws + 24576, gqo);
    u32x4 w6[8], G1[8], G2[8];
    uint64_t mR;
    asm("s_cmp_eq_u32 %1, 0\n\ts_cselect_b64 %0, -1, 0" : "=s"(mR) : "s"(hh) : "scc");
    const u32 o1 = (u32)(rh * 128 + 32 * (n >> 3) + 8 * g4) * 1152 + 128 * w + 16 * (n & 7);
    const int u5 = n >= 10 ? 2 : (n >= 5 ? 1 : 0);
    int cb = n - 5 * u5; if (cb > ncb - 1) cb = ncb - 1;
    const int s6 = w < 6 ? w : w - 6;
    const char* p6 = isR ? (const char*)wuv : (w < 6 ? (const char*)wg : (const char*)wu);
    const u32 st6 = isR ? 8192u : 9216u;
    const u32 o6 = isR ? (u32)(256 * rch + 32 * w + 8 * g4) * 8192 + rh * 256 + n * 16 : (u32)(96 * s6 + 32 * u5 + 8 * g4) * 9216 + (fbase + cb) * 16;
#define ROLE_LOAD(k) do { if ((k) < 8) gld(w6[(k) & 7], p6 + ((k) & 7) * st6, o6); else if ((k) < 16) gldm(G1[(k) & 7], (const char*)wo + ((k) & 7) * 1152, o1, mR); \
        else gldm(G2[(k) & 7], (const char*)wo + (64 + ((k) & 7)) * 1152, o1, mR); } while (0)
#define WAITGQ(N) asm volatile("s_waitcnt vmcnt(%4)" : "+v"(gq[0]), "+v"(gq[1]), "+v"(gq[2]), "+v"(gq[3]) : "n"(N) : "memory")
    static_assert(SB <= 8 && SD >= 8, "window loads must be unconditional; the FFN set is requested before the O stores");
    #pragma unroll
    for (int k = 0; k < SA; k++) ROLE_LOAD(k);
    WAITGQ(SA);
    STAMP(16);
    {
        uint64_t m0_ = badtag(gq[0], Ta, Tb), m1_ = badtag(gq[1], Ta, Tb), m2_ = badtag(gq[2], Ta, Tb), m3_ = badtag(gq[3], Ta, Tb);
        const bool any = (m0_ | m1_ | m2_ | m3_) != 0;
        gldcm(gq[0], ws, gqo, m0_); gldcm(gq[1], ws + 8192, gqo, m1_); gldcm(gq[2], ws + 16384, gqo, m2_); gldcm(gq[3], ws + 24576, gqo, m3_);
        #pragma unroll
        for (int k = SA; k < SB; k++) ROLE_LOAD(k);
        if (any) {
            WAITGQ(SB - SA);
            for (int it = 0; it < SPIN_MAX; it++) {
                m0_ = badtag(gq[0], Ta, Tb); m1_ = badtag(gq[1], Ta, Tb); m2_ = badtag(gq[2], Ta, Tb); m3_ = badtag(gq[3], Ta, Tb);
                if ((m0_ | m1_ | m2_ | m3_) == 0) break;
                gldcm(gq[0], ws, gqo, m0_); gldcm(gq[1], ws + 8192, gqo, m1_); gldcm(gq[2], ws + 16384, gqo, m2_); gldcm(gq[3], ws + 24576, gqo, m3_);
                asm volatile("s_waitcnt vmcnt(0)" : "+v"(gq[0]), "+v"(gq[1]), "+v"(gq[2]), "+v"(gq[3])::"memory");
            }
        }
    }
    { const u32 u = 64 * (w & 1) + l;
      #pragma unroll
      for (int k = 0; k < 4; k++) { const u32 hd = 4 * k + (w >> 1); lw<u32x2>(L, L_QA + hd * 1024 + (((u >> 1) ^ hd) << 4) + 8 * (u & 1), u32x2{gq[k][0], gq[k][2]}); } }
    BAR();
    STAMP(4);
    const int pi4 = (g4 == 0) ? 2 : (g4 == 3 ? 3 : g4 - 1);
    f32x4 s4 = {0.f, 0.f, 0.f, 0.f};
    {
        const int nq = n >> 2, pin = (nq == 0) ? 2 : (nq == 3 ? 3 : nq - 1);
        const u32 kap = 16 * w + 4 * pin + (n & 3), sg = ((n & 3) << 2) | pin;
        f32x4 s4b = {0.f, 0.f, 0.f, 0.f};
#define KA(ks) lr<u32x4>(L, L_KV + kap * 1024 + ((ks) >> 2) * 256 + (((4 * ((ks) & 3) + g4) ^ sg) << 4))
#define KB(ks) lr<u32x4>(L, L_QA + n * 1024 + ((ks) >> 2) * 256 + (((4 * ((ks) & 3) + g4) ^ n) << 4))
        u32x4 a0 = KA(0), b0 = KB(0), a1 = KA(1), b1 = KB(1);
        #pragma unroll
        for (int ks = 0; ks < 16; ks += 2) {
            u32x4 na0 = a0, nb0 = b0, na1 = a1, nb1 = b1;
            if (ks < 14) { na0 = KA(ks + 2); nb0 = KB(ks + 2); na1 = KA(ks + 3); nb1 = KB(ks + 3); }
            #pragma unroll
            for (int k = SB + ((SC - SB) * (ks >> 1)) / 8; k < SB + ((SC - SB) * ((ks >> 1) + 1)) / 8; k++) ROLE_LOAD(k);
            s4 = mfma(a0, b0, s4); s4b = mfma(a1, b1, s4b);
            a0 = na0; b0 = nb0; a1 = na1; b1 = nb1;
        }
        s4 += s4b;
    }
    { float m = fmaxf(fmaxf(s4[0], s4[1]), fmaxf(s4[2], s4[3])); m = sw16_max(m); m = sw32_max(m);
      if (g4 == 0) lw<float>(L, L_WM + n * 32 + w * 4, m); }
    BAR();
    float mx;
    { const f32x4 a = lr<f32x4>(L, L_WM + n * 32), b = lr<f32x4>(L, L_WM + n * 32 + 16);
      mx = fmaxf(fmaxf(fmaxf(a[0], a[1]), fmaxf(a[2], a[3])), fmaxf(fmaxf(b[0], b[1]), fmaxf(b[2], b[3])));
      float p[4], s = 0.f;
      #pragma unroll
      for (int k = 0; k < 4; k++) { p[k] = __builtin_amdgcn_exp2f(s4[k] - mx); s += p[k]; }
      lw<u32x2>(L, L_P + n * 272 + (16 * w + 4 * pi4) * 2, u32x2{pk(p[0], p[1]), pk(p[2], p[3])});
      s = sw16_add(s, s); s = sw32_add(s, s);
      if (g4 == 0) lw<float>(L, L_WS + n * 32 + w * 4, s); }
    BAR();
    STAMP(5);
    u32 tm[4];
    #pragma unroll
    for (int k = 0; k < 4; k++) tm[k] = ((Ta >> (2 * k)) & 1u) | (((Ta >> (2 * k + 1)) & 1u) << 16);
    {
        f32x4 o4[4];
        #pragma unroll
        for (int ct = 0; ct < 4; ct++) o4[ct] = f32x4{0.f, 0.f, 0.f, 0.f};
        const u32 q = n >> 2, p = n & 3;
        const u32 xl = (8 * (w & 1)) ^ (p >> 1) ^ (q << 2) ^ ((2 * g4) & 3);
        const u32 vb = L_KV + (8 * g4 + q) * 1024 + (w >> 1) * 256 + 8 * (p & 1);
#define PVRD(ks, PB, V) do { PB = lr<u32x4>(L, L_P + n * 272 + (32 * (ks) + 8 * g4) * 2); _Pragma("unroll") for (int ct = 0; ct < 4; ct++) { \
                const u32x2 v0 = __builtin_bit_cast(u32x2, __builtin_amdgcn_ds_read_tr16_b64_v4i16((LAS s16x4*)(L + vb + (ks) * 32768 + ((xl ^ (2 * ct)) << 4)))); \
                const u32x2 v1 = __builtin_bit_cast(u32x2, __builtin_amdgcn_ds_read_tr16_b64_v4i16((LAS s16x4*)(L + vb + (ks) * 32768 + 4096 + ((xl ^ (2 * ct) ^ 1) << 4)))); \
                V[ct] = u32x4{v0[0], v0[1], v1[0], v1[1]}; } } while (0)
        u32x4 pb, vv[4];
        PVRD(0, pb, vv);
        #pragma unroll
        for (int ks = 0; ks < 4; ks++) {
            u32x4 npb = pb, nv[4] = {vv[0], vv[1], vv[2], vv[3]};
            if (ks < 3) PVRD(ks + 1, npb, nv);
            #pragma unroll
            for (int k = SC + ((SD - SC) * ks) / 4; k < SC + ((SD - SC) * (ks + 1)) / 4; k++) ROLE_LOAD(k);
            #pragma unroll
            for (int ct = 0; ct < 4; ct++) { o4[ct] = mfma(vv[ct], pb, o4[ct]); vv[ct] = nv[ct]; }
            pb = npb;
        }
        const u32 uperm = (g4 >> 1) | ((g4 & 1) << 1);
        const u32 oaddr = WS_O + ((((u32)(ab * 32 + 16 * hh + (n & 7)) * 8 + w) * 32 + chunk) * 128) + ((n >> 3) * 4 + uperm) * 16;
        u32x4 un[2];
        #pragma unroll
        for (int a = 0; a < 2; a++) {
            u32 r[8];
            #pragma unroll
            for (int k = 0; k < 4; k++) { r[k] = asu(o4[2 * a][k]) + 0x10000u; r[4 + k] = asu(o4[2 * a + 1][k]) + 0x10000u; }
            const u32 ax = __builtin_amdgcn_perm(r[1], r[0], 0x07060302u), ay = __builtin_amdgcn_perm(r[3], r[2], 0x07060302u);
            const u32 bx = __builtin_amdgcn_perm(r[5], r[4], 0x07060302u), by = __builtin_amdgcn_perm(r[7], r[6], 0x07060302u);
            auto sx = __builtin_amdgcn_permlane16_swap(ax, bx, false, false);
            auto sy = __builtin_amdgcn_permlane16_swap(ay, by, false, false);
            un[a] = u32x4{(sx[0] & 0xFFFEFFFEu) | tm[0], (sy[0] & 0xFFFEFFFEu) | tm[1], (sx[1] & 0xFFFEFFFEu) | tm[2], (sy[1] & 0xFFFEFFFEu) | tm[3]};
        }
        u32x4 ua_, ub_;
        #pragma unroll
        for (int k = 0; k < 4; k++) {
            const u32 rv = __builtin_amdgcn_update_dpp(0u, n < 8 ? un[1][k] : un[0][k], ROR(8), 0xf, 0xf, true);
            ua_[k] = n < 8 ? un[0][k] : rv; ub_[k] = n < 8 ? rv : un[1][k];
        }
        gstc(ws, oaddr, ua_); gstc(ws + 262144, oaddr, ub_);
    }
    if (w == 0) {
        const f32x4 a = lr<f32x4>(L, L_WS + n * 32), b = lr<f32x4>(L, L_WS + n * 32 + 16);
        const float s = ((a[0] + a[1]) + (a[2] + a[3])) + ((b[0] + b[1]) + (b[2] + b[3]));
        const u32x4 unit = {asu(mx), Ta, asu(s), Tb};
        if (l < 16) gstc(ws, WS_ML + ((u32)(ab * 32 + 16 * hh + n) * 32 + chunk) * 16, unit);
    }
    STAMP(6);
    if (isR) {
        u32x4 we0, we1, og[4], ml;
        #pragma unroll
        for (int k = SD; k < 24; k++) ROLE_LOAD(k);
        { const u32 oe = (u32)(rh * 128 + 16 * w + 4 * g4 + 2 * (n >> 3)) * 1152 + 1024 + 16 * (n & 7);
          gld(we0, wo, oe); gld<1152>(we1, wo, oe); }
        const int bq = w & 1;
        const u32 ogo = WS_O + (((u32)((2 * rbp + bq) * 32 + rh) * 8 + 4 * rch + (w >> 1)) * 32) * 128 + l * 16;
        const u32 mlo = WS_ML + (((u32)((2 * rbp + (l >> 5)) * 32 + rh) * 32) + (l & 31)) * 16;
        gldc(og[0], ws, ogo); gldc<1024>(og[1], ws, ogo); gldc<2048>(og[2], ws, ogo); gldc<3072>(og[3], ws, ogo);
        gldc(ml, ws, mlo);
        RAWBAR();
        u32x4 T0[8], T1[8], T2[8]; u32 TE[8];
        { Sel S; if (SD < 24) { WAITSEL(7, S); } else { WAITSEL(9, S); }
          TRANS8(T0, w6, S); TIE8(T0); TRANS8(T1, G1, S); TIE8(T1); TRANS8(T2, G2, S); TIE8(T2); }
        { Sel S; WAITSEL(5, S); TRANS2(TE, we0, we1, S); TIE8(TE); }
#define WAITOG() asm volatile("s_waitcnt vmcnt(0)" : "+v"(og[0]), "+v"(og[1]), "+v"(og[2]), "+v"(og[3]), "+v"(ml)::"memory")
        WAITOG();
        STAMP(17);
        for (int it = 0; it < SPIN_MAX; it++) {
            uint64_t mk[4], mm = badtag(ml, Ta, Tb), all = mm;
            #pragma unroll
            for (int k = 0; k < 4; k++) {
                mk[k] = line_mask(__builtin_amdgcn_ballot_w64(((((og[k][0] ^ tm[0]) | (og[k][1] ^ tm[1])) | ((og[k][2] ^ tm[2]) | (og[k][3] ^ tm[3]))) & 0x00010001u) != 0));
                all |= mk[k]; }
            if (all == 0) break;
            gldcm(og[0], ws, ogo, mk[0]); gldcm<1024>(og[1], ws, ogo, mk[1]); gldcm<2048>(og[2], ws, ogo, mk[2]); gldcm<3072>(og[3], ws, ogo, mk[3]);
            gldcm(ml, ws, mlo, mm);
            WAITOG();
        }
        STAMP(7);
        {
            const float m = asf(ml[0]);
            float M = row_max(m); M = sw16_max(M);
            const float al = __builtin_amdgcn_exp2f(m - M);
            float Ls = row_sum(al * asf(ml[2])); Ls = sw16_add(Ls, Ls);
            const float scl = al / Ls;
            float a[8] = {0.f, 0.f, 0.f, 0.f, 0.f, 0.f, 0.f, 0.f};
            #pragma unroll
            for (int qd = 0; qd < 4; qd++) {
                const float sc_ = asf(__builtin_amdgcn_ds_bpermute((32 * bq + 8 * qd + (l >> 3)) * 4, asu(scl)));
                #pragma unroll
                for (int k = 0; k < 4; k++) { a[2 * k] += sc_ * asf((og[qd][k] << 16) & 0xFFFE0000u); a[2 * k + 1] += sc_ * asf(og[qd][k] & 0xFFFE0000u); }
            }
            float y[4];
            #pragma unroll
            for (int k = 0; k < 4; k++) y[k] = sw32_add(a[k], a[k + 4]);
            float z0 = sw16_add(y[0], y[2]), z1 = sw16_add(y[1], y[3]);
            z0 += dpp<ROR(8)>(z0); z1 += dpp<ROR(8)>(z1);
            if ((l & 8) == 0) lw<u32>(L, L_RO + bq * 512 + (64 * (w >> 1) + 8 * (l & 7) + 4 * (l >> 5) + 2 * ((l >> 4) & 1)) * 2, pk(z0, z1));
        }
        BAR();
        ZERO8(acc);
        { const u32x4 A = lr<u32x4>(L, L_RO + ((n >> 2) & 1) * 512 + w * 64 + 16 * g4); PRE_MFMA(acc, A, T0); }
        if (g4 < 2) { const u32 o = L_RP + (w * 2 + g4) * 512 + n * 32;
            lw<f32x4>(L, o, f32x4{acc[0][0], acc[1][0], acc[2][0], acc[3][0]}); lw<f32x4>(L, o + 16, f32x4{acc[4][0], acc[5][0], acc[6][0], acc[7][0]}); }
        BAR();
        { const int tt = t & 255; float s = 0.f;
          #pragma unroll
          for (int k = 0; k < 8; k++) s += lr<float>(L, L_RP + k * 1024 + tt * 4);
          const float o = dpp<0xB1>(s);
          if (t < 256 && !(t & 1)) lw<u32>(L, L_RC + t * 2, pk(s, o)); }
        BAR();
        ZERO8(acc);
        { const u32 co = L_RC + ((n >> 2) & 1) * 256 + (32 * (n & 1) + 8 * g4) * 2;
          const u32x4 A0 = lr<u32x4>(L, co), A1 = lr<u32x4>(L, co + 128);
          PRE_MFMA(acc, A0, T1); PRE_MFMA(acc, A1, T2); }
        {
            f32x4 a, b;
            #pragma unroll
            for (int k = 0; k < 4; k++) { a[k] = acc[k][0] + dpp<ROR(8)>(acc[k][1]); b[k] = acc[4 + k][0] + dpp<ROR(8)>(acc[4 + k][1]); }
            if (n < 8 && g4 < 2) { const u32 o = L_RS + g4 * 2304 + (64 * w + 8 * n) * 4; lw<f32x4>(L, o, a); lw<f32x4>(L, o + 16, b); }
        }
        ZERO8(acc);
        { const u32x4 A = {lr<u32>(L, L_RC + ((n >> 2) & 1) * 256 + (16 * w + 4 * g4 + 2 * (n & 1)) * 2), 0u, 0u, 0u};
          PRE_MFMA2(acc, A, TE);
          f32x4 a, b;
          #pragma unroll
          for (int k = 0; k < 4; k++) { a[k] = acc[k][0] + dpp<ROR(8)>(acc[k][1]); b[k] = acc[4 + k][0] + dpp<ROR(8)>(acc[4 + k][1]); }
          if (n < 8 && g4 < 2) { const u32 o = L_RE + (w * 2 + g4) * 256 + n * 32; lw<f32x4>(L, o, a); lw<f32x4>(L, o + 16, b); } }
        BAR();
        #pragma unroll
        for (int r = 0; r < 2; r++) {
            const int u = t + 512 * r;
            if (r == 0 || t < 64) {
                const int bq = u >= 288, c2 = u - 288 * bq;
                f32x2 v;
                if (c2 < 256) v = lr<f32x2>(L, L_RS + bq * 2304 + c2 * 8);
                else { v = f32x2{0.f, 0.f};
                    #pragma unroll
                    for (int k = 0; k < 8; k++) v += lr<f32x2>(L, L_RE + (k * 2 + bq) * 256 + (c2 - 256) * 8); }
                gstc(ws, WS_SP + rid * 9216 + u * 16, u32x4{asu(v[0]), Ta, asu(v[1]), Tb});
            }
        }
        STAMP(8);
    } else {
        u32x4 ua[8], db[8];
        if (w < 4) { const u32 o = (u32)(96 * (2 + w) + 32 * u5 + 8 * g4) * 9216 + (fbase + cb) * 16;
            #pragma unroll
            for (int r = 0; r < 8; r++) gld(ua[r], (const char*)wu + r * 9216, o); }
        BAR();
        if (w != 2 && w != 3) {
            const int rg4 = ncb == 5 ? 4 : 3;
            u32 o;
            if (w >= 4) o = (u32)(8 * fbase + 8 * g4) * 1152 + (16 * (w - 4) + n) * 16;
            else if (w == 0) o = (u32)(8 * fbase + 8 * (n < 8 ? g4 : rg4)) * 1152 + (64 + (n & 7)) * 16;
            else o = (u32)(8 * fbase + 8 * rg4) * 1152 + (16 * g4 + n) * 16;
            #pragma unroll
            for (int r = 0; r < 8; r++) gld(db[r], (const char*)wd + r * 1152, o);
        }
        Sel SF;
        if (w < 2) { WAITSEL(20, SF); } else { WAITSEL(12, SF); }
        const int ua5 = (n >> 2) < 2 ? (n >> 2) : 2;
        const bool fvalid = n < 15 && g4 == u5;
        {
            ZERO8(acc);
            const u32x4 A = lr<u32x4>(L, L_XN + (n & 3) * 1152 + (96 * s6 + 32 * ua5 + 8 * g4) * 2);
            SET_MFMA(acc, A, w6, SF);
            if (fvalid) { const u32 o = L_FPART + ((w < 6 ? 0 : F_GU) + (3 * s6 + u5) * 160 + 8 * (n - 5 * u5)) * 4;
                #pragma unroll
                for (int b = 0; b < 4; b++) { lw<f32x4>(L, o + b * 160, f32x4{acc[0][b], acc[1][b], acc[2][b], acc[3][b]}); lw<f32x4>(L, o + b * 160 + 16, f32x4{acc[4][b], acc[5][b], acc[6][b], acc[7][b]}); } }
        }
        if (w < 4) {
            Sel SU; if (w < 2) { WAITSEL(8, SU); } else { WAITSEL(0, SU); }
            ZERO8(acc);
            const u32x4 A = lr<u32x4>(L, L_XN + (n & 3) * 1152 + (96 * (2 + w) + 32 * ua5 + 8 * g4) * 2);
            SET_MFMA(acc, A, ua, SU);
            if (fvalid) { const u32 o = L_FPART + (F_GU + (3 * (2 + w) + u5) * 160 + 8 * (n - 5 * u5)) * 4;
                #pragma unroll
                for (int b = 0; b < 4; b++) { lw<f32x4>(L, o + b * 160, f32x4{acc[0][b], acc[1][b], acc[2][b], acc[3][b]}); lw<f32x4>(L, o + b * 160 + 16, f32x4{acc[4][b], acc[5][b], acc[6][b], acc[7][b]}); } }
        }
        STAMP(7);
        BAR();
        if (t < 320) {
            const int gu = t & 1, bc = t >> 1;
            float s = 0.f;
            #pragma unroll
            for (int k = 0; k < 18; k++) s += lr<float>(L, L_FPART + (gu * F_GU + k * 160 + bc) * 4);
            const float uu = dpp<0xB1>(s);
            const int b = bc / 40, col = bc - 40 * b;
            const float inner = 0.7978845608028654f * (s + 0.044715f * s * s * s);
            const float e = __expf(2.f * inner);
            const float th = 1.f - 2.f / (e + 1.f);
            float hv = 0.5f * s * (1.f + th) * uu;
            if (col >= 8 * ncb) hv = 0.f;
            if (gu == 0) lw<u16>(L, L_FH + b * 96 + col * 2, (u16)(pk(hv, 0.f) & 0xFFFFu));
        }
        BAR();
        if (w != 2 && w != 3) {
            Sel SDn; WAITSEL(0, SDn);
            ZERO8(acc);
            const int b = n >> 2, rg = n & 3;
            const u32x4 hm = lr<u32x4>(L, L_FH + b * 96 + 16 * g4), he = lr<u32x4>(L, L_FH + b * 96 + 64);
            const u32x4 z4 = {0u, 0u, 0u, 0u};
            u32x4 A;
            if (w >= 4) A = hm;
            else if (w == 0) A = rg == 0 ? hm : ((rg == 1 && g4 == 0) ? he : z4);
            else A = (g4 == rg) ? he : z4;
            SET_MFMA(acc, A, db, SDn);
            if (w >= 4) { const u32 o = L_F0 + g4 * 2304 + (16 * (w - 4) + n) * 32;
                lw<f32x4>(L, o, f32x4{acc[0][0], acc[1][0], acc[2][0], acc[3][0]}); lw<f32x4>(L, o + 16, f32x4{acc[4][0], acc[5][0], acc[6][0], acc[7][0]}); }
            else if (w == 0) {
                f32x4 a, c;
                #pragma unroll
                for (int k = 0; k < 4; k++) { a[k] = acc[k][0] + dpp<ROR(8)>(acc[k][1]); c[k] = acc[4 + k][0] + dpp<ROR(8)>(acc[4 + k][1]); }
                if (n < 8) { const u32 o = L_F0 + g4 * 2304 + (64 + n) * 32; lw<f32x4>(L, o, a); lw<f32x4>(L, o + 16, c); }
            } else {
                #pragma unroll
                for (int u = 0; u < 4; u++) { const u32 o = L_F1 + g4 * 2048 + (16 * u + n) * 32;
                    lw<f32x4>(L, o, f32x4{acc[0][u], acc[1][u], acc[2][u], acc[3][u]}); lw<f32x4>(L, o + 16, f32x4{acc[4][u], acc[5][u], acc[6][u], acc[7][u]}); }
            }
        }
        BAR();
        {
            #pragma unroll
            for (int r = 0; r < 3; r++) {
                const int u = t + 512 * r;
                if (r < 2 || t < 128) {
                    const int b = u / 288, c2 = u - 288 * b;
                    f32x2 v = lr<f32x2>(L, L_F0 + u * 8);
                    if (c2 < 256) v += lr<f32x2>(L, L_F1 + b * 2048 + c2 * 8);
                    gstc(ws, WS_FP + f * 18432 + u * 16, u32x4{asu(v[0]), Ta, asu(v[1]), Tb});
                }
            }
        }
        STAMP(8);
        if (rho < 9) {
            const int fin = 9 * x8 + rho, fb = fin / 18, cg = fin - 18 * fb;
            const int p = t >> 4, c = t & 15;
            const u32 so = WS_SP + (u32)(16 * (p >> 3) + 4 * ((p >> 1) & 3) + 2 * (fb >> 1) + (p & 1)) * 9216 + (fb & 1) * 4608 + cg * 256 + c * 16;
            const u32 fo = WS_FP + (u32)p * 18432 + fb * 4608 + cg * 256 + c * 16;
            u32x4 fa[4], sa[2]; u32 xv;
            gld_u16(xv, x, (fb * 576 + cg * 32 + (t & 31)) * 2);
            #pragma unroll
            for (int r = 0; r < 4; r++) gldc(fa[r], ws + r * 32 * 18432, fo);
            gldc(sa[0], ws, so); gldc(sa[1], ws + 64 * 9216, so);
            asm volatile("s_waitcnt vmcnt(0)" : "+v"(fa[0]), "+v"(fa[1]), "+v"(fa[2]), "+v"(fa[3]), "+v"(sa[0]), "+v"(sa[1]), "+v"(xv)::"memory");
            for (int it = 0; it < SPIN_MAX; it++) {
                uint64_t mf[4], ms0 = badtag(sa[0], Ta, Tb), ms1 = badtag(sa[1], Ta, Tb), all = ms0 | ms1;
                #pragma unroll
                for (int r = 0; r < 4; r++) { mf[r] = badtag(fa[r], Ta, Tb); all |= mf[r]; }
                if (all == 0) break;
                #pragma unroll
                for (int r = 0; r < 4; r++) gldcm(fa[r], ws + r * 32 * 18432, fo, mf[r]);
                gldcm(sa[0], ws, so, ms0); gldcm(sa[1], ws + 64 * 9216, so, ms1);
                asm volatile("s_waitcnt vmcnt(0)" : "+v"(fa[0]), "+v"(fa[1]), "+v"(fa[2]), "+v"(fa[3]), "+v"(sa[0]), "+v"(sa[1])::"memory");
            }
            if (fin == 0 && t == 0) gstc1(ws, WS_E, Ta);
            STAMP(9);
            const float s0 = asf(sa[0][0]) + asf(sa[1][0]), s1 = asf(sa[0][2]) + asf(sa[1][2]);
            const float f0 = (asf(fa[0][0]) + asf(fa[1][0])) + (asf(fa[2][0]) + asf(fa[3][0])), f1 = (asf(fa[0][2]) + asf(fa[1][2])) + (asf(fa[2][2]) + asf(fa[3][2]));
            const float y0 = sw32_add(s0, f0), y1 = sw32_add(s1, f1);
            const float z = sw16_add(y0, y1);
            lw<float>(L, L_FR + w * 256 + (l >> 5) * 128 + (2 * (l & 15) + ((l >> 4) & 1)) * 4, z);
            BAR();
            if (t < 64) {
                float s = 0.f;
                #pragma unroll
                for (int k = 0; k < 8; k++) s += lr<float>(L, L_FR + k * 256 + t * 4);
                auto r = __builtin_amdgcn_permlane32_swap(asu(s), asu(s), false, false);
                const float ss = asf(r[0]), ff = asf(r[1]);
                const float gate = ff / (1.f + __expf(-ff));
                const u32 res = pk(asf(xv << 16) + ss * gate, 0.f) & 0xFFFFu;
                if (t < 32) gst_u16(out, (fb * 576 + cg * 32 + t) * 2, res);
            }
            STAMP(10);
        }
    }
}

extern "C" void kernel_launch(void* const* d_in, const int* in_sizes, int n_in, void* d_out, int out_size, void* d_ws, size_t ws_size, hipStream_t stream) {
    (void)in_sizes; (void)n_in; (void)out_size; (void)ws_size;
    (void)hipFuncSetAttribute((const void*)mla_ffn_fused, hipFuncAttributeMaxDynamicSharedMemorySize, L_TOT);
    mla_ffn_fused<<<256, 512, L_TOT, stream>>>((const u16*)d_in[0], (const u16*)d_in[1], (const u16*)d_in[2], (const u16*)d_in[3], (const u16*)d_in[4],
        (const u16*)d_in[5], (const u16*)d_in[7], (const u16*)d_in[8], (const u16*)d_in[9], (char*)d_ws, (u16*)d_out);
}
```

```cpp
#define CPF 22400
#define RG 1
#define FD 35
#define SA 4
#define SB 4
#define SC 8
#define SD 12
#include <hip/hip_runtime.h>
#include <stdint.h>

typedef uint32_t u32;
typedef uint16_t u16;
typedef u32 u32x4 __attribute__((ext_vector_type(4)));
typedef u32 u32x2 __attribute__((ext_vector_type(2)));
typedef float f32x4 __attribute__((ext_vector_type(4)));
typedef float f32x2 __attribute__((ext_vector_type(2)));
typedef __bf16 bf16x8 __attribute__((ext_vector_type(8)));
typedef __bf16 bf16x2 __attribute__((ext_vector_type(2)));
typedef short s16x4 __attribute__((ext_vector_type(4)));
#define LAS __attribute__((address_space(3)))
#define DI __device__ __forceinline__

constexpr u32 WS_E  = 0;
constexpr u32 WS_QA = 4096;
constexpr u32 WS_O  = WS_QA + 4 * 32 * 128 * 16;
constexpr u32 WS_ML = WS_O + 4 * 32 * 8 * 32 * 128;
constexpr u32 WS_SP = WS_ML + 4 * 32 * 32 * 16;
constexpr u32 WS_FP = WS_SP + 128 * 576 * 16;
constexpr u32 WS_ST = WS_FP + 128 * 1152 * 16;
constexpr u32 L_KV = 0;
constexpr u32 L_QA = 131072;
constexpr u32 L_XN = 147456;
constexpr u32 L_Q  = 152064;
constexpr u32 L_P  = 153088;
constexpr u32 L_WM = 157440;
constexpr u32 L_WS = 157952;
constexpr u32 L_END = 158464;
constexpr u32 L_RO = 0;
constexpr u32 L_RP = 1024;
constexpr u32 L_RC = 9216;
constexpr u32 L_RS = 9728;
constexpr u32 L_RE = 14336;
constexpr u32 L_FR = 18432;
constexpr u32 F_GU = 2896;
constexpr u32 L_FPART = 0;
constexpr u32 L_FH = 23552;
constexpr u32 L_F0 = 24576;
constexpr u32 L_F1 = 33792;

#define HOLDX 0
#define FW 0
#define QG 0
#define KVN 4
constexpr u32 TAGX = 0xA5A5A5A4u;
constexpr int SPIN_MAX = 1 << 16;

DI float asf(u32 v) { return __builtin_bit_cast(float, v); }
DI u32 asu(float v) { return __builtin_bit_cast(u32, v); }
DI float bflo(u32 v) { return asf(v << 16); }
DI float bfhi(u32 v) { return asf(v & 0xFFFF0000u); }
DI u32 pk(float a, float b) { f32x2 v = {a, b}; return __builtin_bit_cast(u32, __builtin_convertvector(v, bf16x2)); }
DI f32x4 mfma(u32x4 a, u32x4 b, f32x4 c) {
    return __builtin_amdgcn_mfma_f32_16x16x32_bf16(__builtin_bit_cast(bf16x8, a), __builtin_bit_cast(bf16x8, b), c, 0, 0, 0);
}
template <int CTRL> DI float dpp(float v) { return asf(__builtin_amdgcn_update_dpp(0, asu(v), CTRL, 0xf, 0xf, true)); }
#define ROR(n) (0x120 + (n))
#define SHL(n) (0x100 + (n))
DI float sw16_add(float a, float b) { auto r = __builtin_amdgcn_permlane16_swap(asu(a), asu(b), false, false); return asf(r[0]) + asf(r[1]); }
DI float sw32_add(float a, float b) { auto r = __builtin_amdgcn_permlane32_swap(asu(a), asu(b), false, false); return asf(r[0]) + asf(r[1]); }
DI float sw16_max(float a) { auto r = __builtin_amdgcn_permlane16_swap(asu(a), asu(a), false, false); return fmaxf(asf(r[0]), asf(r[1])); }
DI float sw32_max(float a) { auto r = __builtin_amdgcn_permlane32_swap(asu(a), asu(a), false, false); return fmaxf(asf(r[0]), asf(r[1])); }
DI float row_sum(float v) { v += dpp<ROR(8)>(v); v += dpp<ROR(4)>(v); v += dpp<ROR(2)>(v); v += dpp<ROR(1)>(v); return v; }
DI float row_max(float v) { v = fmaxf(v, dpp<ROR(8)>(v)); v = fmaxf(v, dpp<ROR(4)>(v)); v = fmaxf(v, dpp<ROR(2)>(v)); v = fmaxf(v, dpp<ROR(1)>(v)); return v; }
DI float wave_sum(float v) { v = row_sum(v); v = sw16_add(v, v); v = sw32_add(v, v); return v; }

#define POL ""
#define POLKV ""
template <int OFF = 0> DI void gld(u32x4& v, const void* sb, u32 vo) { asm volatile("global_load_dwordx4 %0, %1, %2 offset:%3" POL : "=v"(v) : "v"(vo), "s"(sb), "n"(OFF) : "memory"); }
template <int OFF = 0> DI void gldc(u32x4& v, const void* sb, u32 vo) { asm volatile("global_load_dwordx4 %0, %1, %2 offset:%3 sc1" : "=v"(v) : "v"(vo), "s"(sb), "n"(OFF) : "memory"); }
template <int OFF = 0> DI void gldcm(u32x4& v, const void* sb, u32 vo, uint64_t m) { uint64_t sv;
    asm volatile("s_mov_b64 %1, exec\n\ts_mov_b64 exec, %4\n\tglobal_load_dwordx4 %0, %2, %3 offset:%5 sc1\n\ts_mov_b64 exec, %1" : "+v"(v), "=&s"(sv) : "v"(vo), "s"(sb), "s"(m), "n"(OFF) : "memory"); }
DI void gldm(u32x4& v, const void* sb, u32 vo, uint64_t m) { uint64_t sv;
    asm volatile("s_mov_b64 %1, exec\n\ts_mov_b64 exec, %4\n\tglobal_load_dwordx4 %0, %2, %3" POL "\n\ts_mov_b64 exec, %1" : "=v"(v), "=&s"(sv) : "v"(vo), "s"(sb), "s"(m) : "memory"); }
DI uint64_t badtag(const u32x4& v, u32 Ta, u32 Tb) { return __builtin_amdgcn_ballot_w64(v[1] != Ta || v[3] != Tb); }
DI uint64_t line_mask(uint64_t m) { uint64_t b = m | (m >> 4); b |= b >> 2; b |= b >> 1; b &= 0x0101010101010101ull; return (b << 8) - b; }
DI void gld1(u32& v, const void* sb, u32 vo) { asm volatile("global_load_dword %0, %1, %2" : "=v"(v) : "v"(vo), "s"(sb) : "memory"); }
DI void gld_u16(u32& v, const void* sb, u32 vo) { asm volatile("global_load_ushort %0, %1, %2" : "=v"(v) : "v"(vo), "s"(sb) : "memory"); }
template <int OFF = 0> DI void gstc(const void* sb, u32 vo, u32x4 v) { asm volatile("global_store_dwordx4 %0, %1, %2 offset:%3 sc1\n\ts_nop 1" ::"v"(vo), "v"(v), "s"(sb), "n"(OFF) : "memory"); }
DI void gstc1(const void* sb, u32 vo, u32 v) { asm volatile("global_store_dword %0, %1, %2 sc1\n\ts_nop 1" ::"v"(vo), "v"(v), "s"(sb) : "memory"); }
DI void gst_u16(const void* sb, u32 vo, u32 v) { asm volatile("global_store_short %0, %1, %2\n\ts_nop 1" ::"v"(vo), "v"(v), "s"(sb) : "memory"); }
DI void dma16(const void* sb, u32 vo, u32 ldst) {
    u32 keep;
    asm volatile("s_mov_b32 %0, m0\n\ts_mov_b32 m0, %3\n\ts_nop 0\n\tglobal_load_lds_dwordx4 %1, %2" POLKV "\n\ts_mov_b32 m0, %0" : "=&s"(keep) : "v"(vo), "s"(sb), "s"(ldst) : "memory");
}
#define WAITV(N) asm volatile("s_waitcnt vmcnt(" #N ")" ::: "memory")
#define WAIT1(N, a) asm volatile("s_waitcnt vmcnt(" #N ")" : "+v"(a)::"memory")
#define WAIT2(N, a, b) asm volatile("s_waitcnt vmcnt(" #N ")" : "+v"(a), "+v"(b)::"memory")
#define WAIT3(N, a, b, c) asm volatile("s_waitcnt vmcnt(" #N ")" : "+v"(a), "+v"(b), "+v"(c)::"memory")
#define WAIT8(N, a) asm volatile("s_waitcnt vmcnt(" #N ")" : "+v"(a[0]), "+v"(a[1]), "+v"(a[2]), "+v"(a[3]), "+v"(a[4]), "+v"(a[5]), "+v"(a[6]), "+v"(a[7])::"memory")
#define TIE2(a, b) asm volatile("" : "+v"(a), "+v"(b)::"memory")
#define TIE8(a) asm volatile("" : "+v"(a[0]), "+v"(a[1]), "+v"(a[2]), "+v"(a[3]), "+v"(a[4]), "+v"(a[5]), "+v"(a[6]), "+v"(a[7])::"memory")
#define BAR() asm volatile("s_waitcnt lgkmcnt(0)\n\ts_barrier" ::: "memory")
#define RAWBAR() asm volatile("s_barrier" ::: "memory")

struct Sel { u32 lo, hi; };
#define WAITSEL(N, S) asm volatile("s_waitcnt vmcnt(" #N ")\n\ts_mov_b32 %0, 0x05040100\n\ts_mov_b32 %1, 0x07060302" : "=s"(S.lo), "=s"(S.hi)::"memory")
template <int JJ> DI u32x4 tcol(const u32x4* R, Sel S) {
    const u32 sel = (JJ & 1) ? S.hi : S.lo;
    u32x4 o;
    o[0] = __builtin_amdgcn_perm(R[1][JJ >> 1], R[0][JJ >> 1], sel);
    o[1] = __builtin_amdgcn_perm(R[3][JJ >> 1], R[2][JJ >> 1], sel);
    o[2] = __builtin_amdgcn_perm(R[5][JJ >> 1], R[4][JJ >> 1], sel);
    o[3] = __builtin_amdgcn_perm(R[7][JJ >> 1], R[6][JJ >> 1], sel);
    return o;
}
template <int JJ> DI u32 tcol2(u32x4 r0, u32x4 r1, Sel S) { return __builtin_amdgcn_perm(r1[JJ >> 1], r0[JJ >> 1], (JJ & 1) ? S.hi : S.lo); }
#define SET_MFMA(acc, A, R, S) do { \
    acc[0] = mfma(A, tcol<0>(R, S), acc[0]); acc[1] = mfma(A, tcol<1>(R, S), acc[1]); acc[2] = mfma(A, tcol<2>(R, S), acc[2]); acc[3] = mfma(A, tcol<3>(R, S), acc[3]); \
    acc[4] = mfma(A, tcol<4>(R, S), acc[4]); acc[5] = mfma(A, tcol<5>(R, S), acc[5]); acc[6] = mfma(A, tcol<6>(R, S), acc[6]); acc[7] = mfma(A, tcol<7>(R, S), acc[7]); } while (0)
#define TRANS8(T, R, S) do { T[0] = tcol<0>(R, S); T[1] = tcol<1>(R, S); T[2] = tcol<2>(R, S); T[3] = tcol<3>(R, S); T[4] = tcol<4>(R, S); T[5] = tcol<5>(R, S); T[6] = tcol<6>(R, S); T[7] = tcol<7>(R, S); } while (0)
#define TRANS2(T, r0, r1, S) do { T[0] = tcol2<0>(r0, r1, S); T[1] = tcol2<1>(r0, r1, S); T[2] = tcol2<2>(r0, r1, S); T[3] = tcol2<3>(r0, r1, S); \
    T[4] = tcol2<4>(r0, r1, S); T[5] = tcol2<5>(r0, r1, S); T[6] = tcol2<6>(r0, r1, S); T[7] = tcol2<7>(r0, r1, S); } while (0)
#define PRE_MFMA(acc, A, T) do { _Pragma("unroll") for (int _q = 0; _q < 8; _q++) acc[_q] = mfma(A, T[_q], acc[_q]); } while (0)
#define PRE_MFMA2(acc, A, T) do { _Pragma("unroll") for (int _q = 0; _q < 8; _q++) acc[_q] = mfma(A, u32x4{T[_q], 0u, 0u, 0u}, acc[_q]); } while (0)
#define ZERO8(acc) do { _Pragma("unroll") for (int _q = 0; _q < 8; _q++) acc[_q] = f32x4{0.f, 0.f, 0.f, 0.f}; } while (0)

constexpr u32 L_TOT = L_END;
#define STAMP(k) do { } while (0)

template <class T> DI T lr(LAS char* L, u32 off) { return *(LAS T*)(L + off); }
template <class T> DI void lw(LAS char* L, u32 off, T v) { *(LAS T*)(L + off) = v; }

__global__ __launch_bounds__(512, 2) void mla_ffn_fused(const u16* __restrict__ x, const u16* __restrict__ ckv, const u16* __restrict__ wq,
        const u16* __restrict__ wuk, const u16* __restrict__ wuv, const u16* __restrict__ wo, const u16* __restrict__ wg,
        const u16* __restrict__ wu, const u16* __restrict__ wd, char* __restrict__ ws, u16* __restrict__ out) {
    extern __shared__ __attribute__((aligned(16))) char smem[];
    LAS char* L = (LAS char*)smem;
    const u32 ldsbase = (u32)(uintptr_t)L;
    const int g = blockIdx.x, t = threadIdx.x;
    const int w = __builtin_amdgcn_readfirstlane(t >> 6), l = t & 63, n = l & 15, g4 = l >> 4;
    const int x8 = g & 7, i = g >> 3;
    const int h = 4 * x8 + (i >> 3), j = i & 7;
    const int ab = x8 >> 1, chunk = (x8 & 1) * 16 + (i >> 1), hh = i & 1;
    const bool isR = hh == 0;
    const int rho = i >> 1;
    const int rh = 4 * x8 + (rho >> 2), rbp = (rho >> 1) & 1, rch = rho & 1, rid = 16 * x8 + rho;
    const int f = 16 * x8 + rho, fbase = 9 * (f >> 1) + 5 * (f & 1), ncb = 5 - (f & 1);
    STAMP(0);

    int ph0 = 32 * w + 8 * g4 + 72 * j; if (ph0 >= 576) ph0 -= 576;
    int ph1 = 256 + 32 * w + 8 * g4 + 72 * j; if (ph1 >= 576) ph1 -= 576;
    int phe = 512 + 8 * w + 72 * j; if (phe >= 576) phe -= 576;
    phe += 2 * g4;
    u32 ep, xe; u32x4 xa0, xa1, x0, x1;
    u32 cpf0;
    { uint64_t pc; asm volatile("s_getpc_b64 %0" : "=s"(pc));
      const u32 o = min((u32)(w * 24 + min(l, 23)) * 128u, (u32)CPF);
      asm volatile("global_load_dword %0, %1, %2" : "=&v"(cpf0) : "v"(o), "s"(pc) : "memory"); }
    gld1(ep, ws, WS_E);
    { const u32 xb = (n >> 2) * 1152; gld(xa0, x, xb + ph0 * 2); gld(xa1, x, xb + ph1 * 2); gld1(xe, x, xb + phe * 2); }
    { const int lx = l < 36 ? l : l - 36; const u32 xo = (w & 3) * 1152 + lx * 16; gld(x0, x, xo); gld<576>(x1, x, xo); }
    u32x4 uk0, uk1, qa0[8], qa1[8], qe0, qe1;
    { const u32 o = (u32)(64 * j + 8 * w + (n & 7)) * 8192 + h * 256 + 64 * (n >> 3) + 16 * g4;
      gld(uk0, wuk, o); gld<128>(uk1, wuk, o); }
    { const u32 o = (u32)ph0 * 8192 + h * 256 + n * 16;
      #pragma unroll
      for (int r = 0; r < 8; r++) gld(qa0[r], (const char*)wq + r * 8192, o); }
    { const u32 o = (u32)ph1 * 8192 + h * 256 + n * 16;
      #pragma unroll
      for (int r = 0; r < 8; r++) gld(qa1[r], (const char*)wq + r * 8192, o); }
    { const u32 o = (u32)phe * 8192 + h * 256 + n * 16; gld(qe0, wq, o); gld(qe1, (const char*)wq + 8192, o); }
    asm volatile("s_waitcnt vmcnt(20)" : "+v"(ep), "+v"(xe), "+v"(xa0), "+v"(xa1), "+v"(x0), "+v"(x1)::"memory");
    asm volatile("" : "+v"(cpf0));
    STAMP(11);
    if (w < 4) {
        float v[16];
        #pragma unroll
        for (int k = 0; k < 4; k++) { v[2 * k] = bflo(x0[k]); v[2 * k + 1] = bfhi(x0[k]); v[8 + 2 * k] = bflo(x1[k]); v[9 + 2 * k] = bfhi(x1[k]); }
        float s = 0.f;
        #pragma unroll
        for (int k = 0; k < 16; k++) s += v[k];
        if (l >= 36) s = 0.f;
        const float mean = wave_sum(s) * (1.f / 576.f);
        float q = 0.f;
        #pragma unroll
        for (int k = 0; k < 16; k++) { v[k] -= mean; q += v[k] * v[k]; }
        if (l >= 36) q = 0.f;
        const float rs = 1.f / sqrtf(wave_sum(q) * (1.f / 576.f) + 1e-5f);
        if (l == 0) lw<f32x2>(L, L_WM + w * 8, f32x2{mean, rs});
        if (l < 36) {
            u32x4 a, b;
            #pragma unroll
            for (int k = 0; k < 4; k++) { a[k] = pk(v[2 * k] * rs, v[2 * k + 1] * rs); b[k] = pk(v[8 + 2 * k] * rs, v[9 + 2 * k] * rs); }
            lw<u32x4>(L, L_XN + w * 1152 + l * 16, a); lw<u32x4>(L, L_XN + w * 1152 + 576 + l * 16, b);
        }
    }
    RAWBAR();
    STAMP(1);
    f32x4 acc[8];
    ZERO8(acc);
    const bool onesrow = (n & 3) == 1;
    const u32 ONE2 = 0x3F803F80u;
    Sel S0; WAITSEL(10, S0);
    TIE2(uk0, uk1);
    STAMP(12);
    { const u32x4 A = {onesrow ? ONE2 : xa0[0], onesrow ? ONE2 : xa0[1], onesrow ? ONE2 : xa0[2], onesrow ? ONE2 : xa0[3]}; SET_MFMA(acc, A, qa0, S0); }
    const char* kvb = (const char*)ckv + ((size_t)(ab * 4096 + chunk * 128) + w) * 1024;
    #pragma unroll
    for (int r0 = 0; r0 < KVN; r0++) { const int r = (r0 + 8 * hh) & 15;
        dma16(kvb + r * 8192, (u32)((l ^ (((r & 3) << 2) | (r >> 2))) * 16), ldsbase + L_KV + (16 * w + r) * 1024); }
    Sel S1; asm volatile("s_waitcnt vmcnt(%2)\n\ts_mov_b32 %0, 0x05040100\n\ts_mov_b32 %1, 0x07060302" : "=s"(S1.lo), "=s"(S1.hi) : "n"(KVN) : "memory");
    STAMP(13);
    { const u32x4 A = {onesrow ? ONE2 : xa1[0], onesrow ? ONE2 : xa1[1], onesrow ? ONE2 : xa1[2], onesrow ? ONE2 : xa1[3]}; SET_MFMA(acc, A, qa1, S1); }
    { const u32x4 A = {onesrow ? ONE2 : xe, 0u, 0u, 0u}; u32 TE[8]; TRANS2(TE, qe0, qe1, S1); PRE_MFMA2(acc, A, TE); }
    { const u32 o = L_QA + (w * 4 + g4) * 512 + n * 32;
      lw<f32x4>(L, o, f32x4{acc[0][0], acc[1][0], acc[2][0], acc[3][0]}); lw<f32x4>(L, o + 16, f32x4{acc[4][0], acc[5][0], acc[6][0], acc[7][0]});
      if (g4 == 0) { const u32 oc = L_P + w * 512 + n * 32;
        lw<f32x4>(L, oc, f32x4{acc[0][1], acc[1][1], acc[2][1], acc[3][1]}); lw<f32x4>(L, oc + 16, f32x4{acc[4][1], acc[5][1], acc[6][1], acc[7][1]}); } }
    STAMP(14);
    BAR();
    STAMP(2);
    { float s = 0.f, c = 0.f;
      #pragma unroll
      for (int k = 0; k < 8; k++) { s += lr<float>(L, L_QA + k * 2048 + t * 4); c += lr<float>(L, L_P + k * 512 + (t & 127) * 4); }
      const f32x2 mr = lr<f32x2>(L, L_WM + (t >> 7) * 8);
      s = (s - mr[0] * c) * mr[1];
      const float o = dpp<0xB1>(s);
      if (!(t & 1)) lw<u32>(L, L_Q + t * 2, pk(s, o)); }
    BAR();
    STAMP(15);
    const u32 Ta = ep + 1, Tb = Ta ^ TAGX;
    {
        const u32 qo = L_Q + (n & 3) * 256 + 64 * ((n >> 2) & 1) + 16 * g4;
        f32x4 c = {0.f, 0.f, 0.f, 0.f};
        c = mfma(uk0, lr<u32x4>(L, qo), c);
        c = mfma(uk1, lr<u32x4>(L, qo + 128), c);
        const float sc = 0.08838834764831845f * 1.4426950408889634f;
        float r4[4];
        #pragma unroll
        for (int k = 0; k < 4; k++) { auto p = __builtin_amdgcn_permlane32_swap(asu(c[k]), asu(c[k]), false, false); r4[k] = (c[k] + dpp<SHL(4)>(asf(p[1]))) * sc; }
        const u32x4 unit = {pk(r4[0], r4[1]), Ta, pk(r4[2], r4[3]), Tb};
        if (l < 32 && n < 4) gstc(ws, WS_QA + (((u32)(n * 32 + h) * 128) + 16 * j + 2 * w + g4) * 16, unit);
    }
    STAMP(3);
    #pragma unroll
    for (int r0 = KVN; r0 < 16; r0++) { const int r = (r0 + 8 * hh) & 15;
        dma16(kvb + r * 8192, (u32)((l ^ (((r & 3) << 2) | (r >> 2))) * 16), ldsbase + L_KV + (16 * w + r) * 1024); }
    u32x4 gq[4];
    const u32 gqo = WS_QA + (((u32)(ab * 32 + 16 * hh + (w >> 1)) * 128) + 64 * (w & 1) + l) * 16;
    if (!QG) { gldc(gq[0], ws, gqo); gldc(gq[1], ws + 8192, gqo); gldc(gq[2], ws + 16384, gqo); gldc(gq[3], ws + 24576, gqo); }
    u32x4 w6[8], G1[8], G2[8];
    uint64_t mR;
    asm("s_cmp_eq_u32 %1, 0\n\ts_cselect_b64 %0, -1, 0" : "=s"(mR) : "s"(hh) : "scc");
    const u32 o1 = (u32)(rh * 128 + 32 * (n >> 3) + 8 * g4) * 1152 + 128 * w + 16 * (n & 7);
    const int u5 = n >= 10 ? 2 : (n >= 5 ? 1 : 0);
    int cb = n - 5 * u5; if (cb > ncb - 1) cb = ncb - 1;
    const int s6 = w < 6 ? w : w - 6;
    const char* p6 = isR ? (const char*)wuv : (w < 6 ? (const char*)wg : (const char*)wu);
    const u32 st6 = isR ? 8192u : 9216u;
    const u32 o6 = isR ? (u32)(256 * rch + 32 * w + 8 * g4) * 8192 + rh * 256 + n * 16 : (u32)(96 * s6 + 32 * u5 + 8 * g4) * 9216 + (fbase + cb) * 16;
#define ROLE_LOAD(k) do { if ((k) < 8) { if (FW) gldm(w6[(k) & 7], p6 + ((k) & 7) * st6, o6, mR); else gld(w6[(k) & 7], p6 + ((k) & 7) * st6, o6); } else if ((k) < 16) gldm(G1[(k) & 7], (const char*)wo + ((k) & 7) * 1152, o1, mR); \
        else gldm(G2[(k) & 7], (const char*)wo + (64 + ((k) & 7)) * 1152, o1, mR); } while (0)
#define WAITGQ(N) asm volatile("s_waitcnt vmcnt(%4)" : "+v"(gq[0]), "+v"(gq[1]), "+v"(gq[2]), "+v"(gq[3]) : "n"(N) : "memory")
    static_assert((!RG || SD < 24) && (!FW || SA == SB) && SB <= 8 && SD >= 8, "window loads must be unconditional; the FFN set is requested before the O stores");
    #pragma unroll
    for (int k = 0; k < SA; k++) ROLE_LOAD(k);
    if (QG) { gldc(gq[0], ws, gqo); gldc(gq[1], ws + 8192, gqo); gldc(gq[2], ws + 16384, gqo); gldc(gq[3], ws + 24576, gqo); WAITGQ(0); } else
    if (FW) asm volatile("s_cmp_eq_u32 %5, 0\n\ts_cbranch_scc1 1f\n\ts_waitcnt vmcnt(0)\n1:\n\ts_waitcnt vmcnt(%4)" : "+v"(gq[0]), "+v"(gq[1]), "+v"(gq[2]), "+v"(gq[3]) : "n"(SA), "s"(hh) : "memory", "scc");
    else WAITGQ(SA);
    STAMP(16);
    {
        uint64_t m0_ = badtag(gq[0], Ta, Tb), m1_ = badtag(gq[1], Ta, Tb), m2_ = badtag(gq[2], Ta, Tb), m3_ = badtag(gq[3], Ta, Tb);
        const bool any = (m0_ | m1_ | m2_ | m3_) != 0;
        gldcm(gq[0], ws, gqo, m0_); gldcm(gq[1], ws + 8192, gqo, m1_); gldcm(gq[2], ws + 16384, gqo, m2_); gldcm(gq[3], ws + 24576, gqo, m3_);
        #pragma unroll
        for (int k = SA; k < SB; k++) ROLE_LOAD(k);
        if (any) {
            WAITGQ(SB - SA);
            for (int it = 0; it < SPIN_MAX; it++) {
                m0_ = badtag(gq[0], Ta, Tb); m1_ = badtag(gq[1], Ta, Tb); m2_ = badtag(gq[2], Ta, Tb); m3_ = badtag(gq[3], Ta, Tb);
                if ((m0_ | m1_ | m2_ | m3_) == 0) break;
                gldcm(gq[0], ws, gqo, m0_); gldcm(gq[1], ws + 8192, gqo, m1_); gldcm(gq[2], ws + 16384, gqo, m2_); gldcm(gq[3], ws + 24576, gqo, m3_);
                asm volatile("s_waitcnt vmcnt(0)" : "+v"(gq[0]), "+v"(gq[1]), "+v"(gq[2]), "+v"(gq[3])::"memory");
            }
        }
    }
    { const u32 u = 64 * (w & 1) + l;
      #pragma unroll
      for (int k = 0; k < 4; k++) { const u32 hd = 4 * k + (w >> 1); lw<u32x2>(L, L_QA + hd * 1024 + (((u >> 1) ^ hd) << 4) + 8 * (u & 1), u32x2{gq[k][0], gq[k][2]}); } }
    BAR();
    STAMP(4);
    const int pi4 = (g4 == 0) ? 2 : (g4 == 3 ? 3 : g4 - 1);
    f32x4 s4 = {0.f, 0.f, 0.f, 0.f};
    {
        const int nq = n >> 2, pin = (nq == 0) ? 2 : (nq == 3 ? 3 : nq - 1);
        const u32 kap = 16 * w + 4 * pin + (n & 3), sg = ((n & 3) << 2) | pin;
        f32x4 s4b = {0.f, 0.f, 0.f, 0.f};
#define KA(ks) lr<u32x4>(L, L_KV + kap * 1024 + ((ks) >> 2) * 256 + (((4 * ((ks) & 3) + g4) ^ sg) << 4))
#define KB(ks) lr<u32x4>(L, L_QA + n * 1024 + ((ks) >> 2) * 256 + (((4 * ((ks) & 3) + g4) ^ n) << 4))
        u32x4 a0 = KA(0), b0 = KB(0), a1 = KA(1), b1 = KB(1);
        #pragma unroll
        for (int ks = 0; ks < 16; ks += 2) {
            u32x4 na0 = a0, nb0 = b0, na1 = a1, nb1 = b1;
            if (ks < 14) { na0 = KA(ks + 2); nb0 = KB(ks + 2); na1 = KA(ks + 3); nb1 = KB(ks + 3); }
            #pragma unroll
            for (int k = SB + ((SC - SB) * (ks >> 1)) / 8; k < SB + ((SC - SB) * ((ks >> 1) + 1)) / 8; k++) ROLE_LOAD(k);
            s4 = mfma(a0, b0, s4); s4b = mfma(a1, b1, s4b);
            a0 = na0; b0 = nb0; a1 = na1; b1 = nb1;
        }
        s4 += s4b;
    }
    { float m = fmaxf(fmaxf(s4[0], s4[1]), fmaxf(s4[2], s4[3])); m = sw16_max(m); m = sw32_max(m);
      if (g4 == 0) lw<float>(L, L_WM + n * 32 + w * 4, m); }
    BAR();
    float mx;
    { const f32x4 a = lr<f32x4>(L, L_WM + n * 32), b = lr<f32x4>(L, L_WM + n * 32 + 16);
      mx = fmaxf(fmaxf(fmaxf(a[0], a[1]), fmaxf(a[2], a[3])), fmaxf(fmaxf(b[0], b[1]), fmaxf(b[2], b[3])));
      float p[4], s = 0.f;
      #pragma unroll
      for (int k = 0; k < 4; k++) { p[k] = __builtin_amdgcn_exp2f(s4[k] - mx); s += p[k]; }
      lw<u32x2>(L, L_P + n * 272 + (16 * w + 4 * pi4) * 2, u32x2{pk(p[0], p[1]), pk(p[2], p[3])});
      s = sw16_add(s, s); s = sw32_add(s, s);
      if (g4 == 0) lw<float>(L, L_WS + n * 32 + w * 4, s); }
    BAR();
    STAMP(5);
    u32 tm[4];
    #pragma unroll
    for (int k = 0; k < 4; k++) tm[k] = ((Ta >> (2 * k)) & 1u) | (((Ta >> (2 * k + 1)) & 1u) << 16);
    {
        f32x4 o4[4];
        #pragma unroll
        for (int ct = 0; ct < 4; ct++) o4[ct] = f32x4{0.f, 0.f, 0.f, 0.f};
        const u32 q = n >> 2, p = n & 3;
        const u32 xl = (8 * (w & 1)) ^ (p >> 1) ^ (q << 2) ^ ((2 * g4) & 3);
        const u32 vb = L_KV + (8 * g4 + q) * 1024 + (w >> 1) * 256 + 8 * (p & 1);
#define PVRD(ks, PB, V) do { PB = lr<u32x4>(L, L_P + n * 272 + (32 * (ks) + 8 * g4) * 2); _Pragma("unroll") for (int ct = 0; ct < 4; ct++) { \
                const u32x2 v0 = __builtin_bit_cast(u32x2, __builtin_amdgcn_ds_read_tr16_b64_v4i16((LAS s16x4*)(L + vb + (ks) * 32768 + ((xl ^ (2 * ct)) << 4)))); \
                const u32x2 v1 = __builtin_bit_cast(u32x2, __builtin_amdgcn_ds_read_tr16_b64_v4i16((LAS s16x4*)(L + vb + (ks) * 32768 + 4096 + ((xl ^ (2 * ct) ^ 1) << 4)))); \
                V[ct] = u32x4{v0[0], v0[1], v1[0], v1[1]}; } } while (0)
        u32x4 pb, vv[4];
        PVRD(0, pb, vv);
        #pragma unroll
        for (int ks = 0; ks < 4; ks++) {
            u32x4 npb = pb, nv[4] = {vv[0], vv[1], vv[2], vv[3]};
            if (ks < 3) PVRD(ks + 1, npb, nv);
            #pragma unroll
            for (int k = SC + ((SD - SC) * ks) / 4; k < SC + ((SD - SC) * (ks + 1)) / 4; k++) ROLE_LOAD(k);
            #pragma unroll
            for (int ct = 0; ct < 4; ct++) { o4[ct] = mfma(vv[ct], pb, o4[ct]); vv[ct] = nv[ct]; }
            pb = npb;
        }
        const u32 uperm = (g4 >> 1) | ((g4 & 1) << 1);
        const u32 oaddr = WS_O + ((((u32)(ab * 32 + 16 * hh + (n & 7)) * 8 + w) * 32 + chunk) * 128) + ((n >> 3) * 4 + uperm) * 16;
        u32x4 un[2];
        #pragma unroll
        for (int a = 0; a < 2; a++) {
            u32 r[8];
            #pragma unroll
            for (int k = 0; k < 4; k++) { r[k] = asu(o4[2 * a][k]) + 0x10000u; r[4 + k] = asu(o4[2 * a + 1][k]) + 0x10000u; }
            const u32 ax = __builtin_amdgcn_perm(r[1], r[0], 0x07060302u), ay = __builtin_amdgcn_perm(r[3], r[2], 0x07060302u);
            const u32 bx = __builtin_amdgcn_perm(r[5], r[4], 0x07060302u), by = __builtin_amdgcn_perm(r[7], r[6], 0x07060302u);
            auto sx = __builtin_amdgcn_permlane16_swap(ax, bx, false, false);
            auto sy = __builtin_amdgcn_permlane16_swap(ay, by, false, false);
            un[a] = u32x4{(sx[0] & 0xFFFEFFFEu) | tm[0], (sy[0] & 0xFFFEFFFEu) | tm[1], (sx[1] & 0xFFFEFFFEu) | tm[2], (sy[1] & 0xFFFEFFFEu) | tm[3]};
        }
        u32x4 ua_, ub_;
        #pragma unroll
        for (int k = 0; k < 4; k++) {
            const u32 rv = __builtin_amdgcn_update_dpp(0u, n < 8 ? un[1][k] : un[0][k], ROR(8), 0xf, 0xf, true);
            ua_[k] = n < 8 ? un[0][k] : rv; ub_[k] = n < 8 ? rv : un[1][k];
        }
        gstc(ws, oaddr, ua_); gstc(ws + 262144, oaddr, ub_);
    }
    if (w == 0) {
        const f32x4 a = lr<f32x4>(L, L_WS + n * 32), b = lr<f32x4>(L, L_WS + n * 32 + 16);
        const float s = ((a[0] + a[1]) + (a[2] + a[3])) + ((b[0] + b[1]) + (b[2] + b[3]));
        const u32x4 unit = {asu(mx), Ta, asu(s), Tb};
        if (l < 16) gstc(ws, WS_ML + ((u32)(ab * 32 + 16 * hh + n) * 32 + chunk) * 16, unit);
    }
    STAMP(6);
    if (isR) {
        u32x4 we0, we1, og[4], ml;
        #pragma unroll
        for (int k = SD; k < 24; k++) ROLE_LOAD(k);
        { const u32 oe = (u32)(rh * 128 + 16 * w + 4 * g4 + 2 * (n >> 3)) * 1152 + 1024 + 16 * (n & 7);
          gld(we0, wo, oe); gld<1152>(we1, wo, oe); }
        const int bq = w & 1;
        const u32 ogo = WS_O + (((u32)((2 * rbp + bq) * 32 + rh) * 8 + 4 * rch + (w >> 1)) * 32) * 128 + l * 16;
        const u32 mlo = WS_ML + (((u32)((2 * rbp + (l >> 5)) * 32 + rh) * 32) + (l & 31)) * 16;
#define OGLOADS() do { gldc(og[0], ws, ogo); gldc<1024>(og[1], ws, ogo); gldc<2048>(og[2], ws, ogo); gldc<3072>(og[3], ws, ogo); gldc(ml, ws, mlo); } while (0)
        if (!RG) OGLOADS();
        RAWBAR();
        u32x4 T0[8], T1[8], T2[8]; u32 TE[8];
        { Sel S; if (RG) { WAITSEL(2, S); OGLOADS(); } else if (SD < 24) { WAITSEL(7, S); } else { WAITSEL(9, S); }
          TRANS8(T0, w6, S); TIE8(T0); TRANS8(T1, G1, S); TIE8(T1); TRANS8(T2, G2, S); TIE8(T2); }
        { Sel S; WAITSEL(5, S); TRANS2(TE, we0, we1, S); TIE8(TE); }
#define WAITOG() asm volatile("s_waitcnt vmcnt(0)" : "+v"(og[0]), "+v"(og[1]), "+v"(og[2]), "+v"(og[3]), "+v"(ml)::"memory")
        WAITOG();
        STAMP(17);
        for (int it = 0; it < SPIN_MAX; it++) {
            uint64_t mk[4], mm = badtag(ml, Ta, Tb), all = mm;
            #pragma unroll
            for (int k = 0; k < 4; k++) {
                mk[k] = line_mask(__builtin_amdgcn_ballot_w64(((((og[k][0] ^ tm[0]) | (og[k][1] ^ tm[1])) | ((og[k][2] ^ tm[2]) | (og[k][3] ^ tm[3]))) & 0x00010001u) != 0));
                all |= mk[k]; }
            if (all == 0) break;
            gldcm(og[0], ws, ogo, mk[0]); gldcm<1024>(og[1], ws, ogo, mk[1]); gldcm<2048>(og[2], ws, ogo, mk[2]); gldcm<3072>(og[3], ws, ogo, mk[3]);
            gldcm(ml, ws, mlo, mm);
            WAITOG();
        }
        STAMP(7);
        {
            const float m = asf(ml[0]);
            float M = row_max(m); M = sw16_max(M);
            const float al = __builtin_amdgcn_exp2f(m - M);
            float Ls = row_sum(al * asf(ml[2])); Ls = sw16_add(Ls, Ls);
            const float scl = al / Ls;
            float a[8] = {0.f, 0.f, 0.f, 0.f, 0.f, 0.f, 0.f, 0.f};
            #pragma unroll
            for (int qd = 0; qd < 4; qd++) {
                const float sc_ = asf(__builtin_amdgcn_ds_bpermute((32 * bq + 8 * qd + (l >> 3)) * 4, asu(scl)));
                #pragma unroll
                for (int k = 0; k < 4; k++) { a[2 * k] += sc_ * asf((og[qd][k] << 16) & 0xFFFE0000u); a[2 * k + 1] += sc_ * asf(og[qd][k] & 0xFFFE0000u); }
            }
            float y[4];
            #pragma unroll
            for (int k = 0; k < 4; k++) y[k] = sw32_add(a[k], a[k + 4]);
            float z0 = sw16_add(y[0], y[2]), z1 = sw16_add(y[1], y[3]);
            z0 += dpp<ROR(8)>(z0); z1 += dpp<ROR(8)>(z1);
            if ((l & 8) == 0) lw<u32>(L, L_RO + bq * 512 + (64 * (w >> 1) + 8 * (l & 7) + 4 * (l >> 5) + 2 * ((l >> 4) & 1)) * 2, pk(z0, z1));
        }
        BAR();
        STAMP(9);
        ZERO8(acc);
        { const u32x4 A = lr<u32x4>(L, L_RO + ((n >> 2) & 1) * 512 + w * 64 + 16 * g4); PRE_MFMA(acc, A, T0); }
        if (g4 < 2) { const u32 o = L_RP + (w * 2 + g4) * 512 + n * 32;
            lw<f32x4>(L, o, f32x4{acc[0][0], acc[1][0], acc[2][0], acc[3][0]}); lw<f32x4>(L, o + 16, f32x4{acc[4][0], acc[5][0], acc[6][0], acc[7][0]}); }
        BAR();
        STAMP(10);
        { const int tt = t & 255; float s = 0.f;
          #pragma unroll
          for (int k = 0; k < 8; k++) s += lr<float>(L, L_RP + k * 1024 + tt * 4);
          const float o = dpp<0xB1>(s);
          if (t < 256 && !(t & 1)) lw<u32>(L, L_RC + t * 2, pk(s, o)); }
        BAR();
        STAMP(18);
        ZERO8(acc);
        { const u32 co = L_RC + ((n >> 2) & 1) * 256 + (32 * (n & 1) + 8 * g4) * 2;
          const u32x4 A0 = lr<u32x4>(L, co), A1 = lr<u32x4>(L, co + 128);
          PRE_MFMA(acc, A0, T1); PRE_MFMA(acc, A1, T2); }
        {
            f32x4 a, b;
            #pragma unroll
            for (int k = 0; k < 4; k++) { a[k] = acc[k][0] + dpp<ROR(8)>(acc[k][1]); b[k] = acc[4 + k][0] + dpp<ROR(8)>(acc[4 + k][1]); }
            if (n < 8 && g4 < 2) { const u32 o = L_RS + g4 * 2304 + (64 * w + 8 * n) * 4; lw<f32x4>(L, o, a); lw<f32x4>(L, o + 16, b); }
        }
        ZERO8(acc);
        { const u32x4 A = {lr<u32>(L, L_RC + ((n >> 2) & 1) * 256 + (16 * w + 4 * g4 + 2 * (n & 1)) * 2), 0u, 0u, 0u};
          PRE_MFMA2(acc, A, TE);
          f32x4 a, b;
          #pragma unroll
          for (int k = 0; k < 4; k++) { a[k] = acc[k][0] + dpp<ROR(8)>(acc[k][1]); b[k] = acc[4 + k][0] + dpp<ROR(8)>(acc[4 + k][1]); }
          if (n < 8 && g4 < 2) { const u32 o = L_RE + (w * 2 + g4) * 256 + n * 32; lw<f32x4>(L, o, a); lw<f32x4>(L, o + 16, b); } }
        BAR();
        STAMP(19);
        #pragma unroll
        for (int r = 0; r < 2; r++) {
            const int u = t + 512 * r;
            if (r == 0 || t < 64) {
                const int bq = u >= 288, c2 = u - 288 * bq;
                f32x2 v;
                if (c2 < 256) v = lr<f32x2>(L, L_RS + bq * 2304 + c2 * 8);
                else { v = f32x2{0.f, 0.f};
                    #pragma unroll
                    for (int k = 0; k < 8; k++) v += lr<f32x2>(L, L_RE + (k * 2 + bq) * 256 + (c2 - 256) * 8); }
                gstc(ws, WS_SP + rid * 9216 + u * 16, u32x4{asu(v[0]), Ta, asu(v[1]), Tb});
            }
        }
        STAMP(8);
    } else {
        u32x4 ua[8], db[8];
        if (FD) __builtin_amdgcn_s_sleep(FD);
        if (FW) {
            #pragma unroll
            for (int r = 0; r < 8; r++) gld(w6[r], p6 + r * st6, o6); }
        if (w < 4) { const u32 o = (u32)(96 * (2 + w) + 32 * u5 + 8 * g4) * 9216 + (fbase + cb) * 16;
            #pragma unroll
            for (int r = 0; r < 8; r++) gld(ua[r], (const char*)wu + r * 9216, o); }
        BAR();
        if (w != 2 && w != 3) {
            const int rg4 = ncb == 5 ? 4 : 3;
            u32 o;
            if (w >= 4) o = (u32)(8 * fbase + 8 * g4) * 1152 + (16 * (w - 4) + n) * 16;
            else if (w == 0) o = (u32)(8 * fbase + 8 * (n < 8 ? g4 : rg4)) * 1152 + (64 + (n & 7)) * 16;
            else o = (u32)(8 * fbase + 8 * rg4) * 1152 + (16 * g4 + n) * 16;
            #pragma unroll
            for (int r = 0; r < 8; r++) gld(db[r], (const char*)wd + r * 1152, o);
        }
        Sel SF;
        if (FW) { if (w < 2) { WAITSEL(16, SF); } else { WAITSEL(8, SF); } } else { if (w < 2) { WAITSEL(20, SF); } else { WAITSEL(12, SF); } }
        const int ua5 = (n >> 2) < 2 ? (n >> 2) : 2;
        const bool fvalid = n < 15 && g4 == u5;
        {
            ZERO8(acc);
            const u32x4 A = lr<u32x4>(L, L_XN + (n & 3) * 1152 + (96 * s6 + 32 * ua5 + 8 * g4) * 2);
            SET_MFMA(acc, A, w6, SF);
            if (fvalid) { const u32 o = L_FPART + ((w < 6 ? 0 : F_GU) + (3 * s6 + u5) * 160 + 8 * (n - 5 * u5)) * 4;
                #pragma unroll
                for (int b = 0; b < 4; b++) { lw<f32x4>(L, o + b * 160, f32x4{acc[0][b], acc[1][b], acc[2][b], acc[3][b]}); lw<f32x4>(L, o + b * 160 + 16, f32x4{acc[4][b], acc[5][b], acc[6][b], acc[7][b]}); } }
        }
        if (w < 4) {
            Sel SU; if (w < 2) { WAITSEL(8, SU); } else { WAITSEL(0, SU); }
            ZERO8(acc);
            const u32x4 A = lr<u32x4>(L, L_XN + (n & 3) * 1152 + (96 * (2 + w) + 32 * ua5 + 8 * g4) * 2);
            SET_MFMA(acc, A, ua, SU);
            if (fvalid) { const u32 o = L_FPART + (F_GU + (3 * (2 + w) + u5) * 160 + 8 * (n - 5 * u5)) * 4;
                #pragma unroll
                for (int b = 0; b < 4; b++) { lw<f32x4>(L, o + b * 160, f32x4{acc[0][b], acc[1][b], acc[2][b], acc[3][b]}); lw<f32x4>(L, o + b * 160 + 16, f32x4{acc[4][b], acc[5][b], acc[6][b], acc[7][b]}); } }
        }
        STAMP(7);
        BAR();
        if (t < 320) {
            const int gu = t & 1, bc = t >> 1;
            float s = 0.f;
            #pragma unroll
            for (int k = 0; k < 18; k++) s += lr<float>(L, L_FPART + (gu * F_GU + k * 160 + bc) * 4);
            const float uu = dpp<0xB1>(s);
            const int b = bc / 40, col = bc - 40 * b;
            const float inner = 0.7978845608028654f * (s + 0.044715f * s * s * s);
            const float e = __expf(2.f * inner);
            const float th = 1.f - 2.f / (e + 1.f);
            float hv = 0.5f * s * (1.f + th) * uu;
            if (col >= 8 * ncb) hv = 0.f;
            if (gu == 0) lw<u16>(L, L_FH + b * 96 + col * 2, (u16)(pk(hv, 0.f) & 0xFFFFu));
        }
        BAR();
        if (w != 2 && w != 3) {
            Sel SDn; WAITSEL(0, SDn);
            ZERO8(acc);
            const int b = n >> 2, rg = n & 3;
            const u32x4 hm = lr<u32x4>(L, L_FH + b * 96 + 16 * g4), he = lr<u32x4>(L, L_FH + b * 96 + 64);
            const u32x4 z4 = {0u, 0u, 0u, 0u};
            u32x4 A;
            if (w >= 4) A = hm;
            else if (w == 0) A = rg == 0 ? hm : ((rg == 1 && g4 == 0) ? he : z4);
            else A = (g4 == rg) ? he : z4;
            SET_MFMA(acc, A, db, SDn);
            if (w >= 4) { const u32 o = L_F0 + g4 * 2304 + (16 * (w - 4) + n) * 32;
                lw<f32x4>(L, o, f32x4{acc[0][0], acc[1][0], acc[2][0], acc[3][0]}); lw<f32x4>(L, o + 16, f32x4{acc[4][0], acc[5][0], acc[6][0], acc[7][0]}); }
            else if (w == 0) {
                f32x4 a, c;
                #pragma unroll
                for (int k = 0; k < 4; k++) { a[k] = acc[k][0] + dpp<ROR(8)>(acc[k][1]); c[k] = acc[4 + k][0] + dpp<ROR(8)>(acc[4 + k][1]); }
                if (n < 8) { const u32 o = L_F0 + g4 * 2304 + (64 + n) * 32; lw<f32x4>(L, o, a); lw<f32x4>(L, o + 16, c); }
            } else {
                #pragma unroll
                for (int u = 0; u < 4; u++) { const u32 o = L_F1 + g4 * 2048 + (16 * u + n) * 32;
                    lw<f32x4>(L, o, f32x4{acc[0][u], acc[1][u], acc[2][u], acc[3][u]}); lw<f32x4>(L, o + 16, f32x4{acc[4][u], acc[5][u], acc[6][u], acc[7][u]}); }
            }
        }
        BAR();
        {
            #pragma unroll
            for (int r = 0; r < 3; r++) {
                const int u = t + 512 * r;
                if (r < 2 || t < 128) {
                    const int b = u / 288, c2 = u - 288 * b;
                    f32x2 v = lr<f32x2>(L, L_F0 + u * 8);
                    if (c2 < 256) v += lr<f32x2>(L, L_F1 + b * 2048 + c2 * 8);
                    gstc(ws, WS_FP + f * 18432 + u * 16, u32x4{asu(v[0]), Ta, asu(v[1]), Tb});
                }
            }
        }
        STAMP(8);
        if (rho < 9) {
            const int fin = 9 * x8 + rho, fb = fin / 18, cg = fin - 18 * fb;
            const int p = t >> 4, c = t & 15;
            const u32 so = WS_SP + (u32)(16 * (p >> 3) + 4 * ((p >> 1) & 3) + 2 * (fb >> 1) + (p & 1)) * 9216 + (fb & 1) * 4608 + cg * 256 + c * 16;
            const u32 fo = WS_FP + (u32)p * 18432 + fb * 4608 + cg * 256 + c * 16;
            u32x4 fa[4], sa[2]; u32 xv;
            gld_u16(xv, x, (fb * 576 + cg * 32 + (t & 31)) * 2);
            #pragma unroll
            for (int r = 0; r < 4; r++) gldc(fa[r], ws + r * 32 * 18432, fo);
            gldc(sa[0], ws, so); gldc(sa[1], ws + 64 * 9216, so);
            asm volatile("s_waitcnt vmcnt(0)" : "+v"(fa[0]), "+v"(fa[1]), "+v"(fa[2]), "+v"(fa[3]), "+v"(sa[0]), "+v"(sa[1]), "+v"(xv)::"memory");
            for (int it = 0; it < SPIN_MAX; it++) {
                uint64_t mf[4], ms0 = badtag(sa[0], Ta, Tb), ms1 = badtag(sa[1], Ta, Tb), all = ms0 | ms1;
                #pragma unroll
                for (int r = 0; r < 4; r++) { mf[r] = badtag(fa[r], Ta, Tb); all |= mf[r]; }
                if (all == 0) break;
                #pragma unroll
                for (int r = 0; r < 4; r++) gldcm(fa[r], ws + r * 32 * 18432, fo, mf[r]);
                gldcm(sa[0], ws, so, ms0); gldcm(sa[1], ws + 64 * 9216, so, ms1);
                asm volatile("s_waitcnt vmcnt(0)" : "+v"(fa[0]), "+v"(fa[1]), "+v"(fa[2]), "+v"(fa[3]), "+v"(sa[0]), "+v"(sa[1])::"memory");
            }
            if (fin == 0 && t == 0) gstc1(ws, WS_E, Ta);
            STAMP(9);
            const float s0 = asf(sa[0][0]) + asf(sa[1][0]), s1 = asf(sa[0][2]) + asf(sa[1][2]);
            const float f0 = (asf(fa[0][0]) + asf(fa[1][0])) + (asf(fa[2][0]) + asf(fa[3][0])), f1 = (asf(fa[0][2]) + asf(fa[1][2])) + (asf(fa[2][2]) + asf(fa[3][2]));
            const float y0 = sw32_add(s0, f0), y1 = sw32_add(s1, f1);
            const float z = sw16_add(y0, y1);
            lw<float>(L, L_FR + w * 256 + (l >> 5) * 128 + (2 * (l & 15) + ((l >> 4) & 1)) * 4, z);
            BAR();
            if (t < 64) {
                float s = 0.f;
                #pragma unroll
                for (int k = 0; k < 8; k++) s += lr<float>(L, L_FR + k * 256 + t * 4);
                auto r = __builtin_amdgcn_permlane32_swap(asu(s), asu(s), false, false);
                const float ss = asf(r[0]), ff = asf(r[1]);
                const float gate = ff / (1.f + __expf(-ff));
                const u32 res = pk(asf(xv << 16) + ss * gate, 0.f) & 0xFFFFu;
                if (t < 32) gst_u16(out, (fb * 576 + cg * 32 + t) * 2, res);
            }
            STAMP(10);
        }
    }
    STAMP(20);
}

extern "C" void kernel_launch(void* const* d_in, const int* in_sizes, int n_in, void* d_out, int out_size, void* d_ws, size_t ws_size, hipStream_t stream) {
    (void)in_sizes; (void)n_in; (void)out_size; (void)ws_size;
    (void)hipFuncSetAttribute((const void*)mla_ffn_fused, hipFuncAttributeMaxDynamicSharedMemorySize, L_TOT);
    mla_ffn_fused<<<256, 512, L_TOT, stream>>>((const u16*)d_in[0], (const u16*)d_in[1], (const u16*)d_in[2], (const u16*)d_in[3], (const u16*)d_in[4],
        (const u16*)d_in[5], (const u16*)d_in[7], (const u16*)d_in[8], (const u16*)d_in[9], (char*)d_ws, (u16*)d_out);
}
```
